```python
import torch

def _softmax(x, axis=-1):
    return torch.softmax(x, dim=axis)
def _gelu(x):
    return torch.nn.functional.gelu(x, approximate="tanh")
def _silu(x):
    return torch.nn.functional.silu(x)
def _sigmoid(x):
    return torch.sigmoid(x)
def _layernorm(x, axis=-1, eps=1e-5):
    m = x.mean(dim=axis, keepdim=True)
    v = x.var(dim=axis, keepdim=True, unbiased=False)
    return (x - m) / torch.sqrt(v + eps)
def _scatter_add(tgt, idx, src, axis=0):
    return tgt.clone().index_add_(axis, idx, src)
def _segment_sum(src, seg, n):
    out = torch.zeros((n,) + src.shape[1:], dtype=src.dtype)
    return out.index_add_(0, seg, src)
def _pad(x, pw):
    flat = tuple(v for pair in reversed(pw) for v in pair)
    return torch.nn.functional.pad(x, flat)

kernel_name = 'concat_heads__ssm_conv_s5__mha_block__prompt'

def setup_inputs():

    _w = (lambda a: (lambda t: t.to(torch.bfloat16) if t.is_floating_point() else t)(torch.as_tensor(a)))
    def _A_setup():
        B, S, D = 1, 2048, 448
        P = D
        Lambda_re = -0.5 * (lambda _x: torch.exp(torch.as_tensor(_x)))((lambda *s: torch.randn(*s, dtype=torch.bfloat16) * 0.4)(P))
        Lambda_im = (lambda *s: torch.randn(*s, dtype=torch.bfloat16) * 0.8)(P) * 1.0
        B_re = (lambda *s: torch.randn(*s, dtype=torch.bfloat16) * 0.4)(P, D) * (D ** -0.5)
        B_im = (lambda *s: torch.randn(*s, dtype=torch.bfloat16) * 0.4)(P, D) * (D ** -0.5)
        C_re = (lambda *s: torch.randn(*s, dtype=torch.bfloat16) * 0.4)(D, P) * (P ** -0.5)
        C_im = (lambda *s: torch.randn(*s, dtype=torch.bfloat16) * 0.4)(D, P) * (P ** -0.5)
        log_dt = (lambda *s: torch.randn(*s, dtype=torch.bfloat16) * 0.4)(P) * 0.3 - 2.0
        Dskip = (lambda *s: torch.randn(*s, dtype=torch.bfloat16) * 0.05)(D)
        return {
            "x": _w((lambda *s: torch.randn(*s, dtype=torch.bfloat16) * 0.05)(B, S, D)),
            "Lambda_re": _w(Lambda_re),
            "Lambda_im": _w(Lambda_im),
            "B_re": _w(B_re),
            "B_im": _w(B_im),
            "C_re": _w(C_re),
            "C_im": _w(C_im),
            "log_dt": _w(log_dt),
            "Dskip": _w(Dskip),
        }
    _A_in = _A_setup()

    def _B_setup():
        B, S, D, H = 1, 2048, 448, 16
        return {
            "x": _w((lambda *s: torch.randn(*s, dtype=torch.bfloat16) * 0.2)(B, S, D)),
            "wqkv": _w((lambda *s: torch.randn(*s, dtype=torch.bfloat16) * 0.2)(D, 3 * D)),
            "wo": _w((lambda *s: torch.randn(*s, dtype=torch.bfloat16) * 0.05)(D, D)),
        }
    _B_in = _B_setup()

    return {
        "A_x": _A_in["x"],
        "A_Lambda_re": _A_in["Lambda_re"],
        "A_Lambda_im": _A_in["Lambda_im"],
        "A_B_re": _A_in["B_re"],
        "A_B_im": _A_in["B_im"],
        "A_C_re": _A_in["C_re"],
        "A_C_im": _A_in["C_im"],
        "A_log_dt": _A_in["log_dt"],
        "A_Dskip": _A_in["Dskip"],
        "B_x": _B_in["x"],
        "B_wqkv": _B_in["wqkv"],
        "B_wo": _B_in["wo"],
    }

def reference(A_x, A_Lambda_re, A_Lambda_im, A_B_re, A_B_im, A_C_re, A_C_im, A_log_dt, A_Dskip, B_x, B_wqkv, B_wo):
    def _A_ref(x, Lambda_re, Lambda_im, B_re, B_im, C_re, C_im, log_dt, Dskip):
        Bsz, L, H = x.shape
        Lam = Lambda_re + 1j * Lambda_im
        Bmat = B_re + 1j * B_im
        Cmat = C_re + 1j * C_im
        dt = torch.exp(log_dt)
        Lam_bar = torch.exp(dt * Lam)
        B_bar = ((Lam_bar - 1.0) / Lam)[:, None] * Bmat
        Bu = torch.einsum("bld,pd->blp", x + 0j, B_bar)
        h = torch.zeros_like(Bu[:, 0, :])
        hs = []
        for t in range(L):
            h = Lam_bar[None, :] * h + Bu[:, t, :]
            hs.append(h)
        hseq = torch.stack(hs, 1)
        y = 2.0 * torch.einsum("blp,dp->bld", hseq, Cmat).real + x * Dskip[None, None, :]
        return y

    def _B_ref(x, wqkv, wo):
        B, S, D = x.shape
        H = 16
        Dh = D // H
        qkv = (lambda x, *d: x.permute(*d))((x @ wqkv).reshape(B, S, 3, H, Dh), 2, 0, 3, 1, 4)
        q, k, v = qkv[0], qkv[1], qkv[2]
        scores = torch.einsum("bhqd,bhkd->bhqk", q, k) * (Dh ** -0.5)
        p = _softmax(scores, axis=-1)
        o = torch.einsum("bhqk,bhkd->bhqd", p, v)
        return (lambda x, *d: x.permute(*d))(o, 0, 2, 1, 3).reshape(B, S, D) @ wo

    a = _A_ref(A_x, A_Lambda_re, A_Lambda_im, A_B_re, A_B_im, A_C_re, A_C_im, A_log_dt, A_Dskip)
    b = _B_ref(B_x, B_wqkv, B_wo)
    return torch.concatenate([a, b], axis=-1)
```

```cpp
#include <hip/hip_runtime.h>
#include <stdint.h>
#define ATTN_LOOP_ASM \
  "s_mov_b64 s[56:57], %[kb]\n" \
  "s_mov_b64 s[58:59], %[vb]\n" \
  "s_add_u32 s62, %[wv], 1\n" \
  "s_and_b32 s62, s62, 3\n" \
  "s_lshl_b32 s62, s62, 12\n" \
  "v_add_u32 v144, s62, %[rb]\n" \
  "s_add_u32 s62, %[wv], 2\n" \
  "s_and_b32 s62, s62, 3\n" \
  "s_lshl_b32 s62, s62, 12\n" \
  "v_add_u32 v145, s62, %[rb]\n" \
  "s_add_u32 s62, %[wv], 3\n" \
  "s_and_b32 s62, s62, 3\n" \
  "s_lshl_b32 s62, s62, 12\n" \
  "v_add_u32 v146, s62, %[rb]\n" \
  "s_add_u32 s62, %[wv], 4\n" \
  "s_and_b32 s62, s62, 3\n" \
  "s_lshl_b32 s62, s62, 12\n" \
  "v_add_u32 v147, s62, %[rb]\n" \
  "ds_read_b128 v[240:243], %[rb] offset:2048\n" \
  "ds_read_b128 v[244:247], %[rb] offset:3072\n" \
  "ds_read_b128 v[16:19], v144 offset:0\n" \
  "ds_read_b128 v[20:23], v144 offset:1024\n" \
  "ds_read_b128 v[24:27], v145 offset:0\n" \
  "ds_read_b128 v[28:31], v145 offset:1024\n" \
  "ds_read_b128 v[32:35], v146 offset:0\n" \
  "ds_read_b128 v[36:39], v146 offset:1024\n" \
  "ds_read_b128 v[40:43], v147 offset:0\n" \
  "ds_read_b128 v[44:47], v147 offset:1024\n" \
  "v_mov_b32 v48, 0\n" \
  "v_mov_b32 v49, 0\n" \
  "v_mov_b32 v50, 0\n" \
  "v_mov_b32 v51, 0\n" \
  "v_mov_b32 v52, 0\n" \
  "v_mov_b32 v53, 0\n" \
  "v_mov_b32 v54, 0\n" \
  "v_mov_b32 v55, 0\n" \
  "v_mov_b32 v56, 0\n" \
  "v_mov_b32 v57, 0\n" \
  "v_mov_b32 v58, 0\n" \
  "v_mov_b32 v59, 0\n" \
  "v_mov_b32 v60, 0\n" \
  "v_mov_b32 v61, 0\n" \
  "v_mov_b32 v62, 0\n" \
  "v_mov_b32 v63, 0\n" \
  "v_mov_b32 v64, 0\n" \
  "v_mov_b32 v65, 0\n" \
  "v_mov_b32 v66, 0\n" \
  "v_mov_b32 v67, 0\n" \
  "v_mov_b32 v68, 0\n" \
  "v_mov_b32 v69, 0\n" \
  "v_mov_b32 v70, 0\n" \
  "v_mov_b32 v71, 0\n" \
  "v_mov_b32 v72, 0\n" \
  "v_mov_b32 v73, 0\n" \
  "v_mov_b32 v74, 0\n" \
  "v_mov_b32 v75, 0\n" \
  "v_mov_b32 v76, 0\n" \
  "v_mov_b32 v77, 0\n" \
  "v_mov_b32 v78, 0\n" \
  "v_mov_b32 v79, 0\n" \
  "v_mov_b32 v80, 0\n" \
  "v_mov_b32 v81, 0\n" \
  "v_mov_b32 v82, 0\n" \
  "v_mov_b32 v83, 0\n" \
  "v_mov_b32 v84, 0\n" \
  "v_mov_b32 v85, 0\n" \
  "v_mov_b32 v86, 0\n" \
  "v_mov_b32 v87, 0\n" \
  "v_mov_b32 v88, 0\n" \
  "v_mov_b32 v89, 0\n" \
  "v_mov_b32 v90, 0\n" \
  "v_mov_b32 v91, 0\n" \
  "v_mov_b32 v92, 0\n" \
  "v_mov_b32 v93, 0\n" \
  "v_mov_b32 v94, 0\n" \
  "v_mov_b32 v95, 0\n" \
  "v_mov_b32 v96, 0\n" \
  "v_mov_b32 v97, 0\n" \
  "v_mov_b32 v98, 0\n" \
  "v_mov_b32 v99, 0\n" \
  "v_mov_b32 v100, 0\n" \
  "v_mov_b32 v101, 0\n" \
  "v_mov_b32 v102, 0\n" \
  "v_mov_b32 v103, 0\n" \
  "v_mov_b32 v104, 0\n" \
  "v_mov_b32 v105, 0\n" \
  "v_mov_b32 v106, 0\n" \
  "v_mov_b32 v107, 0\n" \
  "v_mov_b32 v108, 0\n" \
  "v_mov_b32 v109, 0\n" \
  "v_mov_b32 v110, 0\n" \
  "v_mov_b32 v111, 0\n" \
  "v_mov_b32 v220, 0\n" \
  "v_mov_b32 v221, 0\n" \
  "v_mov_b32 v222, 0\n" \
  "v_mov_b32 v223, 0\n" \
  "s_mov_b32 s62, 0\n" \
  "s_mov_b32 s63, -1\n" \
  "s_waitcnt lgkmcnt(0)\n" \
  "v_mfma_f32_32x32x16_bf16 v[112:127], v[240:243], v[16:19], 0\n" \
  "v_mfma_f32_32x32x16_bf16 v[128:143], v[240:243], v[24:27], 0\n" \
  "v_mfma_f32_32x32x16_bf16 v[144:159], v[240:243], v[32:35], 0\n" \
  "v_mfma_f32_32x32x16_bf16 v[224:239], v[240:243], v[40:43], 0\n" \
  "v_mfma_f32_32x32x16_bf16 v[112:127], v[244:247], v[20:23], v[112:127]\n" \
  "v_mfma_f32_32x32x16_bf16 v[128:143], v[244:247], v[28:31], v[128:143]\n" \
  "v_mfma_f32_32x32x16_bf16 v[144:159], v[244:247], v[36:39], v[144:159]\n" \
  "v_mfma_f32_32x32x16_bf16 v[224:239], v[244:247], v[44:47], v[224:239]\n" \
  "s_nop 7\n" \
  "s_nop 3\n" \
  "v_max3_f32 v112, v112, v113, v114\n" \
  "v_max3_f32 v115, v115, v116, v117\n" \
  "v_max3_f32 v118, v118, v119, v120\n" \
  "v_max3_f32 v121, v121, v122, v123\n" \
  "v_max3_f32 v124, v124, v125, v126\n" \
  "v_max3_f32 v112, v112, v115, v127\n" \
  "v_max3_f32 v118, v118, v121, v124\n" \
  "v_max_f32 v112, v112, v118\n" \
  "v_max3_f32 v128, v128, v129, v130\n" \
  "v_max3_f32 v131, v131, v132, v133\n" \
  "v_max3_f32 v134, v134, v135, v136\n" \
  "v_max3_f32 v137, v137, v138, v139\n" \
  "v_max3_f32 v140, v140, v141, v142\n" \
  "v_max3_f32 v128, v128, v131, v143\n" \
  "v_max3_f32 v134, v134, v137, v140\n" \
  "v_max_f32 v128, v128, v134\n" \
  "v_max3_f32 v144, v144, v145, v146\n" \
  "v_max3_f32 v147, v147, v148, v149\n" \
  "v_max3_f32 v150, v150, v151, v152\n" \
  "v_max3_f32 v153, v153, v154, v155\n" \
  "v_max3_f32 v156, v156, v157, v158\n" \
  "v_max3_f32 v144, v144, v147, v159\n" \
  "v_max3_f32 v150, v150, v153, v156\n" \
  "v_max_f32 v144, v144, v150\n" \
  "s_nop 3\n" \
  "v_max3_f32 v224, v224, v225, v226\n" \
  "v_max3_f32 v227, v227, v228, v229\n" \
  "v_max3_f32 v230, v230, v231, v232\n" \
  "v_max3_f32 v233, v233, v234, v235\n" \
  "v_max3_f32 v236, v236, v237, v238\n" \
  "v_max3_f32 v224, v224, v227, v239\n" \
  "v_max3_f32 v230, v230, v233, v236\n" \
  "v_max_f32 v224, v224, v230\n" \
  "v_mov_b32 v113, v112\n" \
  "v_mov_b32 v129, v128\n" \
  "v_mov_b32 v145, v144\n" \
  "v_mov_b32 v225, v224\n" \
  "s_nop 1\n" \
  "v_permlane32_swap_b32 v112, v113\n" \
  "v_permlane32_swap_b32 v128, v129\n" \
  "v_permlane32_swap_b32 v144, v145\n" \
  "v_permlane32_swap_b32 v224, v225\n" \
  "s_nop 1\n" \
  "v_max_f32 v112, v112, v113\n" \
  "v_max_f32 v128, v128, v129\n" \
  "v_max_f32 v144, v144, v145\n" \
  "v_max_f32 v224, v224, v225\n" \
  "v_cvt_pk_bf16_f32 v112, -v112, 0\n" \
  "v_cvt_pk_bf16_f32 v128, -v128, 0\n" \
  "v_cvt_pk_bf16_f32 v144, -v144, 0\n" \
  "v_cvt_pk_bf16_f32 v224, -v224, 0\n" \
  "s_nop 0\n" \
  "v_cndmask_b32 v22, v22, v112, s[62:63]\n" \
  "v_cndmask_b32 v30, v30, v128, s[62:63]\n" \
  "v_cndmask_b32 v38, v38, v144, s[62:63]\n" \
  "v_cndmask_b32 v46, v46, v224, s[62:63]\n" \
  "v_mov_b32 v148, 0\n" \
  "v_mov_b32 v149, 0\n" \
  "v_mov_b32 v150, 0\n" \
  "v_mov_b32 v151, 0\n" \
  "v_mov_b32 v156, 0\n" \
  "v_mov_b32 v157, 0\n" \
  "v_mfma_f32_32x32x16_bf16 v[112:127], v[160:163], v[16:19], 0\n" \
  "v_mfma_f32_32x32x16_bf16 v[112:127], v[164:167], v[20:23], v[112:127]\n" \
  "s_nop 15\n" \
  "v_mfma_f32_32x32x16_bf16 v[128:143], v[160:163], v[24:27], 0\n" \
  "v_cvt_pk_bf16_f32 v158, v148, v149\n" \
  "v_cvt_pk_bf16_f32 v159, v150, v151\n" \
  "v_exp_f32 v144, v112\n" \
  "v_exp_f32 v145, v113\n" \
  "v_exp_f32 v146, v114\n" \
  "v_exp_f32 v147, v115\n" \
  "v_mfma_f32_32x32x16_bf16 v[96:111], v[220:223], v[156:159], v[96:111]\n" \
  "v_cvt_pk_bf16_f32 v152, v144, v145\n" \
  "v_cvt_pk_bf16_f32 v153, v146, v147\n" \
  "v_exp_f32 v148, v116\n" \
  "v_exp_f32 v149, v117\n" \
  "v_exp_f32 v150, v118\n" \
  "v_exp_f32 v151, v119\n" \
  "v_mfma_f32_32x32x16_bf16 v[128:143], v[164:167], v[28:31], v[128:143]\n" \
  "v_cvt_pk_bf16_f32 v154, v148, v149\n" \
  "v_cvt_pk_bf16_f32 v155, v150, v151\n" \
  "v_exp_f32 v144, v120\n" \
  "v_exp_f32 v145, v121\n" \
  "v_exp_f32 v146, v122\n" \
  "v_exp_f32 v147, v123\n" \
  "v_mfma_f32_32x32x16_bf16 v[48:63], v[192:195], v[152:155], v[48:63]\n" \
  "v_cvt_pk_bf16_f32 v156, v144, v145\n" \
  "v_cvt_pk_bf16_f32 v157, v146, v147\n" \
  "v_exp_f32 v148, v124\n" \
  "v_exp_f32 v149, v125\n" \
  "v_exp_f32 v150, v126\n" \
  "v_exp_f32 v151, v127\n" \
  "s_waitcnt vmcnt(0)\n" \
  "v_mov_b32 v249, %[tg]\n" \
  "s_mov_b64 exec, 1\n" \
  "global_store_dword %[fm], v249, %[fb] sc1\n" \
  "s_mov_b64 exec, -1\n" \
  "v_mfma_f32_32x32x16_bf16 v[112:127], v[160:163], v[32:35], 0\n" \
  "v_cvt_pk_bf16_f32 v158, v148, v149\n" \
  "v_cvt_pk_bf16_f32 v159, v150, v151\n" \
  "v_exp_f32 v144, v128\n" \
  "v_exp_f32 v145, v129\n" \
  "v_exp_f32 v146, v130\n" \
  "v_exp_f32 v147, v131\n" \
  "v_mfma_f32_32x32x16_bf16 v[48:63], v[196:199], v[156:159], v[48:63]\n" \
  "v_cvt_pk_bf16_f32 v152, v144, v145\n" \
  "v_cvt_pk_bf16_f32 v153, v146, v147\n" \
  "v_exp_f32 v148, v132\n" \
  "v_exp_f32 v149, v133\n" \
  "v_exp_f32 v150, v134\n" \
  "v_exp_f32 v151, v135\n" \
  "v_mfma_f32_32x32x16_bf16 v[112:127], v[164:167], v[36:39], v[112:127]\n" \
  "v_cvt_pk_bf16_f32 v154, v148, v149\n" \
  "v_cvt_pk_bf16_f32 v155, v150, v151\n" \
  "v_exp_f32 v144, v136\n" \
  "v_exp_f32 v145, v137\n" \
  "v_exp_f32 v146, v138\n" \
  "v_exp_f32 v147, v139\n" \
  "v_mfma_f32_32x32x16_bf16 v[64:79], v[192:195], v[152:155], v[64:79]\n" \
  "v_cvt_pk_bf16_f32 v156, v144, v145\n" \
  "v_cvt_pk_bf16_f32 v157, v146, v147\n" \
  "v_exp_f32 v148, v140\n" \
  "v_exp_f32 v149, v141\n" \
  "v_exp_f32 v150, v142\n" \
  "v_exp_f32 v151, v143\n" \
  "v_mfma_f32_32x32x16_bf16 v[128:143], v[160:163], v[40:43], 0\n" \
  "v_cvt_pk_bf16_f32 v158, v148, v149\n" \
  "v_cvt_pk_bf16_f32 v159, v150, v151\n" \
  "v_exp_f32 v144, v112\n" \
  "v_exp_f32 v145, v113\n" \
  "v_exp_f32 v146, v114\n" \
  "v_exp_f32 v147, v115\n" \
  "v_mfma_f32_32x32x16_bf16 v[64:79], v[196:199], v[156:159], v[64:79]\n" \
  "v_cvt_pk_bf16_f32 v152, v144, v145\n" \
  "v_cvt_pk_bf16_f32 v153, v146, v147\n" \
  "v_exp_f32 v148, v116\n" \
  "v_exp_f32 v149, v117\n" \
  "v_exp_f32 v150, v118\n" \
  "v_exp_f32 v151, v119\n" \
  "v_mfma_f32_32x32x16_bf16 v[128:143], v[164:167], v[44:47], v[128:143]\n" \
  "v_cvt_pk_bf16_f32 v154, v148, v149\n" \
  "v_cvt_pk_bf16_f32 v155, v150, v151\n" \
  "v_exp_f32 v144, v120\n" \
  "v_exp_f32 v145, v121\n" \
  "v_exp_f32 v146, v122\n" \
  "v_exp_f32 v147, v123\n" \
  "v_mfma_f32_32x32x16_bf16 v[80:95], v[192:195], v[152:155], v[80:95]\n" \
  "v_cvt_pk_bf16_f32 v156, v144, v145\n" \
  "v_cvt_pk_bf16_f32 v157, v146, v147\n" \
  "v_exp_f32 v148, v124\n" \
  "v_exp_f32 v149, v125\n" \
  "v_exp_f32 v150, v126\n" \
  "v_exp_f32 v151, v127\n" \
  "s_mov_b32 s62, 0\n" \
  "PL_%=:\n" \
  "global_load_dword v249, %[fa], %[fb] sc1\n" \
  "s_waitcnt vmcnt(0)\n" \
  "v_cmp_ne_u32 vcc, %[tg], v249\n" \
  "s_nop 1\n" \
  "s_and_b32 s63, vcc_lo, %[am]\n" \
  "s_cbranch_scc0 PG_%=\n" \
  "s_add_u32 s62, s62, 1\n" \
  "s_bitcmp1_b32 s62, 17\n" \
  "s_cbranch_scc1 PG_%=\n" \
  "s_sleep 1\n" \
  "s_branch PL_%=\n" \
  "PG_%=:\n" \
  "s_add_u32 %[ko], %[ko], 0x2000\n" \
  "s_and_b32 %[ko], %[ko], 0x1ffff\n" \
  "s_add_u32 %[vo], %[vo], 0x2000\n" \
  "s_and_b32 %[vo], %[vo], 0x1ffff\n" \
  "s_add_u32 %[ko], %[ko], 0x2000\n" \
  "s_and_b32 %[ko], %[ko], 0x1ffff\n" \
  "s_add_u32 s60, s56, %[ko]\n" \
  "s_addc_u32 s61, s57, 0\n" \
  "global_load_dwordx4 v[168:171], %[lo], s[60:61] sc1\n" \
  "global_load_dwordx4 v[172:175], %[lo], s[60:61] offset:1024 sc1\n" \
  "s_add_u32 %[vo], %[vo], 0x2000\n" \
  "s_and_b32 %[vo], %[vo], 0x1ffff\n" \
  "s_add_u32 s60, s58, %[vo]\n" \
  "s_addc_u32 s61, s59, 0\n" \
  "global_load_dwordx4 v[200:203], %[lo], s[60:61] sc1\n" \
  "global_load_dwordx4 v[204:207], %[lo], s[60:61] offset:1024 sc1\n" \
  "s_add_u32 %[ko], %[ko], 0x2000\n" \
  "s_and_b32 %[ko], %[ko], 0x1ffff\n" \
  "s_add_u32 s60, s56, %[ko]\n" \
  "s_addc_u32 s61, s57, 0\n" \
  "global_load_dwordx4 v[176:179], %[lo], s[60:61] sc1\n" \
  "global_load_dwordx4 v[180:183], %[lo], s[60:61] offset:1024 sc1\n" \
  "s_add_u32 %[vo], %[vo], 0x2000\n" \
  "s_and_b32 %[vo], %[vo], 0x1ffff\n" \
  "s_add_u32 s60, s58, %[vo]\n" \
  "s_addc_u32 s61, s59, 0\n" \
  "global_load_dwordx4 v[208:211], %[lo], s[60:61] sc1\n" \
  "global_load_dwordx4 v[212:215], %[lo], s[60:61] offset:1024 sc1\n" \
  "s_add_u32 %[ko], %[ko], 0x2000\n" \
  "s_and_b32 %[ko], %[ko], 0x1ffff\n" \
  "s_add_u32 s60, s56, %[ko]\n" \
  "s_addc_u32 s61, s57, 0\n" \
  "global_load_dwordx4 v[184:187], %[lo], s[60:61] sc1\n" \
  "global_load_dwordx4 v[188:191], %[lo], s[60:61] offset:1024 sc1\n" \
  "s_add_u32 %[vo], %[vo], 0x2000\n" \
  "s_and_b32 %[vo], %[vo], 0x1ffff\n" \
  "s_add_u32 s60, s58, %[vo]\n" \
  "s_addc_u32 s61, s59, 0\n" \
  "global_load_dwordx4 v[216:219], %[lo], s[60:61] sc1\n" \
  "global_load_dwordx4 v[220:223], %[lo], s[60:61] offset:1024 sc1\n" \
  "s_add_u32 %[ko], %[ko], 0x2000\n" \
  "s_and_b32 %[ko], %[ko], 0x1ffff\n" \
  "s_add_u32 s60, s56, %[ko]\n" \
  "s_addc_u32 s61, s57, 0\n" \
  "global_load_dwordx4 v[160:163], %[lo], s[60:61] sc1\n" \
  "global_load_dwordx4 v[164:167], %[lo], s[60:61] offset:1024 sc1\n" \
  "s_mov_b32 %[cnt], 3\n" \
  "s_branch 2f\n" \
  "1:\n" \
  "v_mfma_f32_32x32x16_bf16 v[128:143], v[160:163], v[24:27], 0\n" \
  "v_cvt_pk_bf16_f32 v158, v148, v149\n" \
  "v_cvt_pk_bf16_f32 v159, v150, v151\n" \
  "v_exp_f32 v144, v112\n" \
  "v_exp_f32 v145, v113\n" \
  "v_exp_f32 v146, v114\n" \
  "v_exp_f32 v147, v115\n" \
  "v_mfma_f32_32x32x16_bf16 v[96:111], v[220:223], v[156:159], v[96:111]\n" \
  "v_cvt_pk_bf16_f32 v152, v144, v145\n" \
  "v_cvt_pk_bf16_f32 v153, v146, v147\n" \
  "v_exp_f32 v148, v116\n" \
  "v_exp_f32 v149, v117\n" \
  "v_exp_f32 v150, v118\n" \
  "v_exp_f32 v151, v119\n" \
  "s_add_u32 %[vo], %[vo], 0x2000\n" \
  "s_and_b32 %[vo], %[vo], 0x1ffff\n" \
  "s_add_u32 s60, s58, %[vo]\n" \
  "s_addc_u32 s61, s59, 0\n" \
  "global_load_dwordx4 v[216:219], %[lo], s[60:61] sc1\n" \
  "global_load_dwordx4 v[220:223], %[lo], s[60:61] offset:1024 sc1\n" \
  "v_mfma_f32_32x32x16_bf16 v[128:143], v[164:167], v[28:31], v[128:143]\n" \
  "v_cvt_pk_bf16_f32 v154, v148, v149\n" \
  "v_cvt_pk_bf16_f32 v155, v150, v151\n" \
  "v_exp_f32 v144, v120\n" \
  "v_exp_f32 v145, v121\n" \
  "v_exp_f32 v146, v122\n" \
  "v_exp_f32 v147, v123\n" \
  "s_waitcnt vmcnt(12)\n" \
  "v_mfma_f32_32x32x16_bf16 v[48:63], v[192:195], v[152:155], v[48:63]\n" \
  "v_cvt_pk_bf16_f32 v156, v144, v145\n" \
  "v_cvt_pk_bf16_f32 v157, v146, v147\n" \
  "v_exp_f32 v148, v124\n" \
  "v_exp_f32 v149, v125\n" \
  "v_exp_f32 v150, v126\n" \
  "v_exp_f32 v151, v127\n" \
  "v_mfma_f32_32x32x16_bf16 v[112:127], v[160:163], v[32:35], 0\n" \
  "v_cvt_pk_bf16_f32 v158, v148, v149\n" \
  "v_cvt_pk_bf16_f32 v159, v150, v151\n" \
  "v_exp_f32 v144, v128\n" \
  "v_exp_f32 v145, v129\n" \
  "v_exp_f32 v146, v130\n" \
  "v_exp_f32 v147, v131\n" \
  "v_mfma_f32_32x32x16_bf16 v[48:63], v[196:199], v[156:159], v[48:63]\n" \
  "v_cvt_pk_bf16_f32 v152, v144, v145\n" \
  "v_cvt_pk_bf16_f32 v153, v146, v147\n" \
  "v_exp_f32 v148, v132\n" \
  "v_exp_f32 v149, v133\n" \
  "v_exp_f32 v150, v134\n" \
  "v_exp_f32 v151, v135\n" \
  "v_mfma_f32_32x32x16_bf16 v[112:127], v[164:167], v[36:39], v[112:127]\n" \
  "v_cvt_pk_bf16_f32 v154, v148, v149\n" \
  "v_cvt_pk_bf16_f32 v155, v150, v151\n" \
  "v_exp_f32 v144, v136\n" \
  "v_exp_f32 v145, v137\n" \
  "v_exp_f32 v146, v138\n" \
  "v_exp_f32 v147, v139\n" \
  "v_mfma_f32_32x32x16_bf16 v[64:79], v[192:195], v[152:155], v[64:79]\n" \
  "v_cvt_pk_bf16_f32 v156, v144, v145\n" \
  "v_cvt_pk_bf16_f32 v157, v146, v147\n" \
  "v_exp_f32 v148, v140\n" \
  "v_exp_f32 v149, v141\n" \
  "v_exp_f32 v150, v142\n" \
  "v_exp_f32 v151, v143\n" \
  "v_mfma_f32_32x32x16_bf16 v[128:143], v[160:163], v[40:43], 0\n" \
  "v_cvt_pk_bf16_f32 v158, v148, v149\n" \
  "v_cvt_pk_bf16_f32 v159, v150, v151\n" \
  "v_exp_f32 v144, v112\n" \
  "v_exp_f32 v145, v113\n" \
  "v_exp_f32 v146, v114\n" \
  "v_exp_f32 v147, v115\n" \
  "v_mfma_f32_32x32x16_bf16 v[64:79], v[196:199], v[156:159], v[64:79]\n" \
  "v_cvt_pk_bf16_f32 v152, v144, v145\n" \
  "v_cvt_pk_bf16_f32 v153, v146, v147\n" \
  "v_exp_f32 v148, v116\n" \
  "v_exp_f32 v149, v117\n" \
  "v_exp_f32 v150, v118\n" \
  "v_exp_f32 v151, v119\n" \
  "v_mfma_f32_32x32x16_bf16 v[128:143], v[164:167], v[44:47], v[128:143]\n" \
  "v_cvt_pk_bf16_f32 v154, v148, v149\n" \
  "v_cvt_pk_bf16_f32 v155, v150, v151\n" \
  "v_exp_f32 v144, v120\n" \
  "v_exp_f32 v145, v121\n" \
  "v_exp_f32 v146, v122\n" \
  "v_exp_f32 v147, v123\n" \
  "s_add_u32 %[ko], %[ko], 0x2000\n" \
  "s_and_b32 %[ko], %[ko], 0x1ffff\n" \
  "s_add_u32 s60, s56, %[ko]\n" \
  "s_addc_u32 s61, s57, 0\n" \
  "global_load_dwordx4 v[160:163], %[lo], s[60:61] sc1\n" \
  "global_load_dwordx4 v[164:167], %[lo], s[60:61] offset:1024 sc1\n" \
  "v_mfma_f32_32x32x16_bf16 v[80:95], v[192:195], v[152:155], v[80:95]\n" \
  "v_cvt_pk_bf16_f32 v156, v144, v145\n" \
  "v_cvt_pk_bf16_f32 v157, v146, v147\n" \
  "v_exp_f32 v148, v124\n" \
  "v_exp_f32 v149, v125\n" \
  "v_exp_f32 v150, v126\n" \
  "v_exp_f32 v151, v127\n" \
  "2:\n" \
  "s_waitcnt vmcnt(12)\n" \
  "v_mfma_f32_32x32x16_bf16 v[112:127], v[168:171], v[16:19], 0\n" \
  "v_cvt_pk_bf16_f32 v158, v148, v149\n" \
  "v_cvt_pk_bf16_f32 v159, v150, v151\n" \
  "v_exp_f32 v144, v128\n" \
  "v_exp_f32 v145, v129\n" \
  "v_exp_f32 v146, v130\n" \
  "v_exp_f32 v147, v131\n" \
  "v_mfma_f32_32x32x16_bf16 v[80:95], v[196:199], v[156:159], v[80:95]\n" \
  "v_cvt_pk_bf16_f32 v152, v144, v145\n" \
  "v_cvt_pk_bf16_f32 v153, v146, v147\n" \
  "v_exp_f32 v148, v132\n" \
  "v_exp_f32 v149, v133\n" \
  "v_exp_f32 v150, v134\n" \
  "v_exp_f32 v151, v135\n" \
  "v_mfma_f32_32x32x16_bf16 v[112:127], v[172:175], v[20:23], v[112:127]\n" \
  "v_cvt_pk_bf16_f32 v154, v148, v149\n" \
  "v_cvt_pk_bf16_f32 v155, v150, v151\n" \
  "v_exp_f32 v144, v136\n" \
  "v_exp_f32 v145, v137\n" \
  "v_exp_f32 v146, v138\n" \
  "v_exp_f32 v147, v139\n" \
  "v_mfma_f32_32x32x16_bf16 v[96:111], v[192:195], v[152:155], v[96:111]\n" \
  "v_cvt_pk_bf16_f32 v156, v144, v145\n" \
  "v_cvt_pk_bf16_f32 v157, v146, v147\n" \
  "v_exp_f32 v148, v140\n" \
  "v_exp_f32 v149, v141\n" \
  "v_exp_f32 v150, v142\n" \
  "v_exp_f32 v151, v143\n" \
  "v_mfma_f32_32x32x16_bf16 v[128:143], v[168:171], v[24:27], 0\n" \
  "v_cvt_pk_bf16_f32 v158, v148, v149\n" \
  "v_cvt_pk_bf16_f32 v159, v150, v151\n" \
  "v_exp_f32 v144, v112\n" \
  "v_exp_f32 v145, v113\n" \
  "v_exp_f32 v146, v114\n" \
  "v_exp_f32 v147, v115\n" \
  "v_mfma_f32_32x32x16_bf16 v[96:111], v[196:199], v[156:159], v[96:111]\n" \
  "v_cvt_pk_bf16_f32 v152, v144, v145\n" \
  "v_cvt_pk_bf16_f32 v153, v146, v147\n" \
  "v_exp_f32 v148, v116\n" \
  "v_exp_f32 v149, v117\n" \
  "v_exp_f32 v150, v118\n" \
  "v_exp_f32 v151, v119\n" \
  "s_add_u32 %[vo], %[vo], 0x2000\n" \
  "s_and_b32 %[vo], %[vo], 0x1ffff\n" \
  "s_add_u32 s60, s58, %[vo]\n" \
  "s_addc_u32 s61, s59, 0\n" \
  "global_load_dwordx4 v[192:195], %[lo], s[60:61] sc1\n" \
  "global_load_dwordx4 v[196:199], %[lo], s[60:61] offset:1024 sc1\n" \
  "v_mfma_f32_32x32x16_bf16 v[128:143], v[172:175], v[28:31], v[128:143]\n" \
  "v_cvt_pk_bf16_f32 v154, v148, v149\n" \
  "v_cvt_pk_bf16_f32 v155, v150, v151\n" \
  "v_exp_f32 v144, v120\n" \
  "v_exp_f32 v145, v121\n" \
  "v_exp_f32 v146, v122\n" \
  "v_exp_f32 v147, v123\n" \
  "s_waitcnt vmcnt(12)\n" \
  "v_mfma_f32_32x32x16_bf16 v[48:63], v[200:203], v[152:155], v[48:63]\n" \
  "v_cvt_pk_bf16_f32 v156, v144, v145\n" \
  "v_cvt_pk_bf16_f32 v157, v146, v147\n" \
  "v_exp_f32 v148, v124\n" \
  "v_exp_f32 v149, v125\n" \
  "v_exp_f32 v150, v126\n" \
  "v_exp_f32 v151, v127\n" \
  "v_mfma_f32_32x32x16_bf16 v[112:127], v[168:171], v[32:35], 0\n" \
  "v_cvt_pk_bf16_f32 v158, v148, v149\n" \
  "v_cvt_pk_bf16_f32 v159, v150, v151\n" \
  "v_exp_f32 v144, v128\n" \
  "v_exp_f32 v145, v129\n" \
  "v_exp_f32 v146, v130\n" \
  "v_exp_f32 v147, v131\n" \
  "v_mfma_f32_32x32x16_bf16 v[48:63], v[204:207], v[156:159], v[48:63]\n" \
  "v_cvt_pk_bf16_f32 v152, v144, v145\n" \
  "v_cvt_pk_bf16_f32 v153, v146, v147\n" \
  "v_exp_f32 v148, v132\n" \
  "v_exp_f32 v149, v133\n" \
  "v_exp_f32 v150, v134\n" \
  "v_exp_f32 v151, v135\n" \
  "v_mfma_f32_32x32x16_bf16 v[112:127], v[172:175], v[36:39], v[112:127]\n" \
  "v_cvt_pk_bf16_f32 v154, v148, v149\n" \
  "v_cvt_pk_bf16_f32 v155, v150, v151\n" \
  "v_exp_f32 v144, v136\n" \
  "v_exp_f32 v145, v137\n" \
  "v_exp_f32 v146, v138\n" \
  "v_exp_f32 v147, v139\n" \
  "v_mfma_f32_32x32x16_bf16 v[64:79], v[200:203], v[152:155], v[64:79]\n" \
  "v_cvt_pk_bf16_f32 v156, v144, v145\n" \
  "v_cvt_pk_bf16_f32 v157, v146, v147\n" \
  "v_exp_f32 v148, v140\n" \
  "v_exp_f32 v149, v141\n" \
  "v_exp_f32 v150, v142\n" \
  "v_exp_f32 v151, v143\n" \
  "v_mfma_f32_32x32x16_bf16 v[128:143], v[168:171], v[40:43], 0\n" \
  "v_cvt_pk_bf16_f32 v158, v148, v149\n" \
  "v_cvt_pk_bf16_f32 v159, v150, v151\n" \
  "v_exp_f32 v144, v112\n" \
  "v_exp_f32 v145, v113\n" \
  "v_exp_f32 v146, v114\n" \
  "v_exp_f32 v147, v115\n" \
  "v_mfma_f32_32x32x16_bf16 v[64:79], v[204:207], v[156:159], v[64:79]\n" \
  "v_cvt_pk_bf16_f32 v152, v144, v145\n" \
  "v_cvt_pk_bf16_f32 v153, v146, v147\n" \
  "v_exp_f32 v148, v116\n" \
  "v_exp_f32 v149, v117\n" \
  "v_exp_f32 v150, v118\n" \
  "v_exp_f32 v151, v119\n" \
  "v_mfma_f32_32x32x16_bf16 v[128:143], v[172:175], v[44:47], v[128:143]\n" \
  "v_cvt_pk_bf16_f32 v154, v148, v149\n" \
  "v_cvt_pk_bf16_f32 v155, v150, v151\n" \
  "v_exp_f32 v144, v120\n" \
  "v_exp_f32 v145, v121\n" \
  "v_exp_f32 v146, v122\n" \
  "v_exp_f32 v147, v123\n" \
  "s_cmp_lg_u32 %[cnt], 3\n" \
  "s_cbranch_scc1 CK_%=\n" \
  "s_mov_b32 s62, 0\n" \
  "CL_%=:\n" \
  "v_cmp_ne_u32 vcc, %[tg], v249\n" \
  "s_nop 1\n" \
  "s_and_b32 s63, vcc_lo, 0xffff\n" \
  "s_cbranch_scc0 CK_%=\n" \
  "s_add_u32 s62, s62, 1\n" \
  "s_bitcmp1_b32 s62, 17\n" \
  "s_cbranch_scc1 CK_%=\n" \
  "s_sleep 1\n" \
  "global_load_dword v249, %[fa], %[fb] sc1\n" \
  "s_waitcnt vmcnt(0)\n" \
  "s_branch CL_%=\n" \
  "CK_%=:\n" \
  "s_add_u32 %[ko], %[ko], 0x2000\n" \
  "s_and_b32 %[ko], %[ko], 0x1ffff\n" \
  "s_add_u32 s60, s56, %[ko]\n" \
  "s_addc_u32 s61, s57, 0\n" \
  "global_load_dwordx4 v[168:171], %[lo], s[60:61] sc1\n" \
  "global_load_dwordx4 v[172:175], %[lo], s[60:61] offset:1024 sc1\n" \
  "v_mfma_f32_32x32x16_bf16 v[80:95], v[200:203], v[152:155], v[80:95]\n" \
  "v_cvt_pk_bf16_f32 v156, v144, v145\n" \
  "v_cvt_pk_bf16_f32 v157, v146, v147\n" \
  "v_exp_f32 v148, v124\n" \
  "v_exp_f32 v149, v125\n" \
  "v_exp_f32 v150, v126\n" \
  "v_exp_f32 v151, v127\n" \
  "s_waitcnt vmcnt(12)\n" \
  "v_mfma_f32_32x32x16_bf16 v[112:127], v[176:179], v[16:19], 0\n" \
  "v_cvt_pk_bf16_f32 v158, v148, v149\n" \
  "v_cvt_pk_bf16_f32 v159, v150, v151\n" \
  "v_exp_f32 v144, v128\n" \
  "v_exp_f32 v145, v129\n" \
  "v_exp_f32 v146, v130\n" \
  "v_exp_f32 v147, v131\n" \
  "v_mfma_f32_32x32x16_bf16 v[80:95], v[204:207], v[156:159], v[80:95]\n" \
  "v_cvt_pk_bf16_f32 v152, v144, v145\n" \
  "v_cvt_pk_bf16_f32 v153, v146, v147\n" \
  "v_exp_f32 v148, v132\n" \
  "v_exp_f32 v149, v133\n" \
  "v_exp_f32 v150, v134\n" \
  "v_exp_f32 v151, v135\n" \
  "v_mfma_f32_32x32x16_bf16 v[112:127], v[180:183], v[20:23], v[112:127]\n" \
  "v_cvt_pk_bf16_f32 v154, v148, v149\n" \
  "v_cvt_pk_bf16_f32 v155, v150, v151\n" \
  "v_exp_f32 v144, v136\n" \
  "v_exp_f32 v145, v137\n" \
  "v_exp_f32 v146, v138\n" \
  "v_exp_f32 v147, v139\n" \
  "v_mfma_f32_32x32x16_bf16 v[96:111], v[200:203], v[152:155], v[96:111]\n" \
  "v_cvt_pk_bf16_f32 v156, v144, v145\n" \
  "v_cvt_pk_bf16_f32 v157, v146, v147\n" \
  "v_exp_f32 v148, v140\n" \
  "v_exp_f32 v149, v141\n" \
  "v_exp_f32 v150, v142\n" \
  "v_exp_f32 v151, v143\n" \
  "v_mfma_f32_32x32x16_bf16 v[128:143], v[176:179], v[24:27], 0\n" \
  "v_cvt_pk_bf16_f32 v158, v148, v149\n" \
  "v_cvt_pk_bf16_f32 v159, v150, v151\n" \
  "v_exp_f32 v144, v112\n" \
  "v_exp_f32 v145, v113\n" \
  "v_exp_f32 v146, v114\n" \
  "v_exp_f32 v147, v115\n" \
  "v_mfma_f32_32x32x16_bf16 v[96:111], v[204:207], v[156:159], v[96:111]\n" \
  "v_cvt_pk_bf16_f32 v152, v144, v145\n" \
  "v_cvt_pk_bf16_f32 v153, v146, v147\n" \
  "v_exp_f32 v148, v116\n" \
  "v_exp_f32 v149, v117\n" \
  "v_exp_f32 v150, v118\n" \
  "v_exp_f32 v151, v119\n" \
  "s_add_u32 %[vo], %[vo], 0x2000\n" \
  "s_and_b32 %[vo], %[vo], 0x1ffff\n" \
  "s_add_u32 s60, s58, %[vo]\n" \
  "s_addc_u32 s61, s59, 0\n" \
  "global_load_dwordx4 v[200:203], %[lo], s[60:61] sc1\n" \
  "global_load_dwordx4 v[204:207], %[lo], s[60:61] offset:1024 sc1\n" \
  "v_mfma_f32_32x32x16_bf16 v[128:143], v[180:183], v[28:31], v[128:143]\n" \
  "v_cvt_pk_bf16_f32 v154, v148, v149\n" \
  "v_cvt_pk_bf16_f32 v155, v150, v151\n" \
  "v_exp_f32 v144, v120\n" \
  "v_exp_f32 v145, v121\n" \
  "v_exp_f32 v146, v122\n" \
  "v_exp_f32 v147, v123\n" \
  "s_waitcnt vmcnt(12)\n" \
  "v_mfma_f32_32x32x16_bf16 v[48:63], v[208:211], v[152:155], v[48:63]\n" \
  "v_cvt_pk_bf16_f32 v156, v144, v145\n" \
  "v_cvt_pk_bf16_f32 v157, v146, v147\n" \
  "v_exp_f32 v148, v124\n" \
  "v_exp_f32 v149, v125\n" \
  "v_exp_f32 v150, v126\n" \
  "v_exp_f32 v151, v127\n" \
  "v_mfma_f32_32x32x16_bf16 v[112:127], v[176:179], v[32:35], 0\n" \
  "v_cvt_pk_bf16_f32 v158, v148, v149\n" \
  "v_cvt_pk_bf16_f32 v159, v150, v151\n" \
  "v_exp_f32 v144, v128\n" \
  "v_exp_f32 v145, v129\n" \
  "v_exp_f32 v146, v130\n" \
  "v_exp_f32 v147, v131\n" \
  "v_mfma_f32_32x32x16_bf16 v[48:63], v[212:215], v[156:159], v[48:63]\n" \
  "v_cvt_pk_bf16_f32 v152, v144, v145\n" \
  "v_cvt_pk_bf16_f32 v153, v146, v147\n" \
  "v_exp_f32 v148, v132\n" \
  "v_exp_f32 v149, v133\n" \
  "v_exp_f32 v150, v134\n" \
  "v_exp_f32 v151, v135\n" \
  "v_mfma_f32_32x32x16_bf16 v[112:127], v[180:183], v[36:39], v[112:127]\n" \
  "v_cvt_pk_bf16_f32 v154, v148, v149\n" \
  "v_cvt_pk_bf16_f32 v155, v150, v151\n" \
  "v_exp_f32 v144, v136\n" \
  "v_exp_f32 v145, v137\n" \
  "v_exp_f32 v146, v138\n" \
  "v_exp_f32 v147, v139\n" \
  "v_mfma_f32_32x32x16_bf16 v[64:79], v[208:211], v[152:155], v[64:79]\n" \
  "v_cvt_pk_bf16_f32 v156, v144, v145\n" \
  "v_cvt_pk_bf16_f32 v157, v146, v147\n" \
  "v_exp_f32 v148, v140\n" \
  "v_exp_f32 v149, v141\n" \
  "v_exp_f32 v150, v142\n" \
  "v_exp_f32 v151, v143\n" \
  "v_mfma_f32_32x32x16_bf16 v[128:143], v[176:179], v[40:43], 0\n" \
  "v_cvt_pk_bf16_f32 v158, v148, v149\n" \
  "v_cvt_pk_bf16_f32 v159, v150, v151\n" \
  "v_exp_f32 v144, v112\n" \
  "v_exp_f32 v145, v113\n" \
  "v_exp_f32 v146, v114\n" \
  "v_exp_f32 v147, v115\n" \
  "v_mfma_f32_32x32x16_bf16 v[64:79], v[212:215], v[156:159], v[64:79]\n" \
  "v_cvt_pk_bf16_f32 v152, v144, v145\n" \
  "v_cvt_pk_bf16_f32 v153, v146, v147\n" \
  "v_exp_f32 v148, v116\n" \
  "v_exp_f32 v149, v117\n" \
  "v_exp_f32 v150, v118\n" \
  "v_exp_f32 v151, v119\n" \
  "v_mfma_f32_32x32x16_bf16 v[128:143], v[180:183], v[44:47], v[128:143]\n" \
  "v_cvt_pk_bf16_f32 v154, v148, v149\n" \
  "v_cvt_pk_bf16_f32 v155, v150, v151\n" \
  "v_exp_f32 v144, v120\n" \
  "v_exp_f32 v145, v121\n" \
  "v_exp_f32 v146, v122\n" \
  "v_exp_f32 v147, v123\n" \
  "s_add_u32 %[ko], %[ko], 0x2000\n" \
  "s_and_b32 %[ko], %[ko], 0x1ffff\n" \
  "s_add_u32 s60, s56, %[ko]\n" \
  "s_addc_u32 s61, s57, 0\n" \
  "global_load_dwordx4 v[176:179], %[lo], s[60:61] sc1\n" \
  "global_load_dwordx4 v[180:183], %[lo], s[60:61] offset:1024 sc1\n" \
  "v_mfma_f32_32x32x16_bf16 v[80:95], v[208:211], v[152:155], v[80:95]\n" \
  "v_cvt_pk_bf16_f32 v156, v144, v145\n" \
  "v_cvt_pk_bf16_f32 v157, v146, v147\n" \
  "v_exp_f32 v148, v124\n" \
  "v_exp_f32 v149, v125\n" \
  "v_exp_f32 v150, v126\n" \
  "v_exp_f32 v151, v127\n" \
  "s_waitcnt vmcnt(12)\n" \
  "v_mfma_f32_32x32x16_bf16 v[112:127], v[184:187], v[16:19], 0\n" \
  "v_cvt_pk_bf16_f32 v158, v148, v149\n" \
  "v_cvt_pk_bf16_f32 v159, v150, v151\n" \
  "v_exp_f32 v144, v128\n" \
  "v_exp_f32 v145, v129\n" \
  "v_exp_f32 v146, v130\n" \
  "v_exp_f32 v147, v131\n" \
  "v_mfma_f32_32x32x16_bf16 v[80:95], v[212:215], v[156:159], v[80:95]\n" \
  "v_cvt_pk_bf16_f32 v152, v144, v145\n" \
  "v_cvt_pk_bf16_f32 v153, v146, v147\n" \
  "v_exp_f32 v148, v132\n" \
  "v_exp_f32 v149, v133\n" \
  "v_exp_f32 v150, v134\n" \
  "v_exp_f32 v151, v135\n" \
  "v_mfma_f32_32x32x16_bf16 v[112:127], v[188:191], v[20:23], v[112:127]\n" \
  "v_cvt_pk_bf16_f32 v154, v148, v149\n" \
  "v_cvt_pk_bf16_f32 v155, v150, v151\n" \
  "v_exp_f32 v144, v136\n" \
  "v_exp_f32 v145, v137\n" \
  "v_exp_f32 v146, v138\n" \
  "v_exp_f32 v147, v139\n" \
  "v_mfma_f32_32x32x16_bf16 v[96:111], v[208:211], v[152:155], v[96:111]\n" \
  "v_cvt_pk_bf16_f32 v156, v144, v145\n" \
  "v_cvt_pk_bf16_f32 v157, v146, v147\n" \
  "v_exp_f32 v148, v140\n" \
  "v_exp_f32 v149, v141\n" \
  "v_exp_f32 v150, v142\n" \
  "v_exp_f32 v151, v143\n" \
  "v_mfma_f32_32x32x16_bf16 v[128:143], v[184:187], v[24:27], 0\n" \
  "v_cvt_pk_bf16_f32 v158, v148, v149\n" \
  "v_cvt_pk_bf16_f32 v159, v150, v151\n" \
  "v_exp_f32 v144, v112\n" \
  "v_exp_f32 v145, v113\n" \
  "v_exp_f32 v146, v114\n" \
  "v_exp_f32 v147, v115\n" \
  "v_mfma_f32_32x32x16_bf16 v[96:111], v[212:215], v[156:159], v[96:111]\n" \
  "v_cvt_pk_bf16_f32 v152, v144, v145\n" \
  "v_cvt_pk_bf16_f32 v153, v146, v147\n" \
  "v_exp_f32 v148, v116\n" \
  "v_exp_f32 v149, v117\n" \
  "v_exp_f32 v150, v118\n" \
  "v_exp_f32 v151, v119\n" \
  "s_add_u32 %[vo], %[vo], 0x2000\n" \
  "s_and_b32 %[vo], %[vo], 0x1ffff\n" \
  "s_add_u32 s60, s58, %[vo]\n" \
  "s_addc_u32 s61, s59, 0\n" \
  "global_load_dwordx4 v[208:211], %[lo], s[60:61] sc1\n" \
  "global_load_dwordx4 v[212:215], %[lo], s[60:61] offset:1024 sc1\n" \
  "v_mfma_f32_32x32x16_bf16 v[128:143], v[188:191], v[28:31], v[128:143]\n" \
  "v_cvt_pk_bf16_f32 v154, v148, v149\n" \
  "v_cvt_pk_bf16_f32 v155, v150, v151\n" \
  "v_exp_f32 v144, v120\n" \
  "v_exp_f32 v145, v121\n" \
  "v_exp_f32 v146, v122\n" \
  "v_exp_f32 v147, v123\n" \
  "s_waitcnt vmcnt(12)\n" \
  "v_mfma_f32_32x32x16_bf16 v[48:63], v[216:219], v[152:155], v[48:63]\n" \
  "v_cvt_pk_bf16_f32 v156, v144, v145\n" \
  "v_cvt_pk_bf16_f32 v157, v146, v147\n" \
  "v_exp_f32 v148, v124\n" \
  "v_exp_f32 v149, v125\n" \
  "v_exp_f32 v150, v126\n" \
  "v_exp_f32 v151, v127\n" \
  "v_mfma_f32_32x32x16_bf16 v[112:127], v[184:187], v[32:35], 0\n" \
  "v_cvt_pk_bf16_f32 v158, v148, v149\n" \
  "v_cvt_pk_bf16_f32 v159, v150, v151\n" \
  "v_exp_f32 v144, v128\n" \
  "v_exp_f32 v145, v129\n" \
  "v_exp_f32 v146, v130\n" \
  "v_exp_f32 v147, v131\n" \
  "v_mfma_f32_32x32x16_bf16 v[48:63], v[220:223], v[156:159], v[48:63]\n" \
  "v_cvt_pk_bf16_f32 v152, v144, v145\n" \
  "v_cvt_pk_bf16_f32 v153, v146, v147\n" \
  "v_exp_f32 v148, v132\n" \
  "v_exp_f32 v149, v133\n" \
  "v_exp_f32 v150, v134\n" \
  "v_exp_f32 v151, v135\n" \
  "v_mfma_f32_32x32x16_bf16 v[112:127], v[188:191], v[36:39], v[112:127]\n" \
  "v_cvt_pk_bf16_f32 v154, v148, v149\n" \
  "v_cvt_pk_bf16_f32 v155, v150, v151\n" \
  "v_exp_f32 v144, v136\n" \
  "v_exp_f32 v145, v137\n" \
  "v_exp_f32 v146, v138\n" \
  "v_exp_f32 v147, v139\n" \
  "v_mfma_f32_32x32x16_bf16 v[64:79], v[216:219], v[152:155], v[64:79]\n" \
  "v_cvt_pk_bf16_f32 v156, v144, v145\n" \
  "v_cvt_pk_bf16_f32 v157, v146, v147\n" \
  "v_exp_f32 v148, v140\n" \
  "v_exp_f32 v149, v141\n" \
  "v_exp_f32 v150, v142\n" \
  "v_exp_f32 v151, v143\n" \
  "v_mfma_f32_32x32x16_bf16 v[128:143], v[184:187], v[40:43], 0\n" \
  "v_cvt_pk_bf16_f32 v158, v148, v149\n" \
  "v_cvt_pk_bf16_f32 v159, v150, v151\n" \
  "v_exp_f32 v144, v112\n" \
  "v_exp_f32 v145, v113\n" \
  "v_exp_f32 v146, v114\n" \
  "v_exp_f32 v147, v115\n" \
  "v_mfma_f32_32x32x16_bf16 v[64:79], v[220:223], v[156:159], v[64:79]\n" \
  "v_cvt_pk_bf16_f32 v152, v144, v145\n" \
  "v_cvt_pk_bf16_f32 v153, v146, v147\n" \
  "v_exp_f32 v148, v116\n" \
  "v_exp_f32 v149, v117\n" \
  "v_exp_f32 v150, v118\n" \
  "v_exp_f32 v151, v119\n" \
  "v_mfma_f32_32x32x16_bf16 v[128:143], v[188:191], v[44:47], v[128:143]\n" \
  "v_cvt_pk_bf16_f32 v154, v148, v149\n" \
  "v_cvt_pk_bf16_f32 v155, v150, v151\n" \
  "v_exp_f32 v144, v120\n" \
  "v_exp_f32 v145, v121\n" \
  "v_exp_f32 v146, v122\n" \
  "v_exp_f32 v147, v123\n" \
  "s_add_u32 %[ko], %[ko], 0x2000\n" \
  "s_and_b32 %[ko], %[ko], 0x1ffff\n" \
  "s_add_u32 s60, s56, %[ko]\n" \
  "s_addc_u32 s61, s57, 0\n" \
  "global_load_dwordx4 v[184:187], %[lo], s[60:61] sc1\n" \
  "global_load_dwordx4 v[188:191], %[lo], s[60:61] offset:1024 sc1\n" \
  "v_mfma_f32_32x32x16_bf16 v[80:95], v[216:219], v[152:155], v[80:95]\n" \
  "v_cvt_pk_bf16_f32 v156, v144, v145\n" \
  "v_cvt_pk_bf16_f32 v157, v146, v147\n" \
  "v_exp_f32 v148, v124\n" \
  "v_exp_f32 v149, v125\n" \
  "v_exp_f32 v150, v126\n" \
  "v_exp_f32 v151, v127\n" \
  "s_waitcnt vmcnt(12)\n" \
  "v_mfma_f32_32x32x16_bf16 v[112:127], v[160:163], v[16:19], 0\n" \
  "v_cvt_pk_bf16_f32 v158, v148, v149\n" \
  "v_cvt_pk_bf16_f32 v159, v150, v151\n" \
  "v_exp_f32 v144, v128\n" \
  "v_exp_f32 v145, v129\n" \
  "v_exp_f32 v146, v130\n" \
  "v_exp_f32 v147, v131\n" \
  "v_mfma_f32_32x32x16_bf16 v[80:95], v[220:223], v[156:159], v[80:95]\n" \
  "v_cvt_pk_bf16_f32 v152, v144, v145\n" \
  "v_cvt_pk_bf16_f32 v153, v146, v147\n" \
  "v_exp_f32 v148, v132\n" \
  "v_exp_f32 v149, v133\n" \
  "v_exp_f32 v150, v134\n" \
  "v_exp_f32 v151, v135\n" \
  "v_mfma_f32_32x32x16_bf16 v[112:127], v[164:167], v[20:23], v[112:127]\n" \
  "v_cvt_pk_bf16_f32 v154, v148, v149\n" \
  "v_cvt_pk_bf16_f32 v155, v150, v151\n" \
  "v_exp_f32 v144, v136\n" \
  "v_exp_f32 v145, v137\n" \
  "v_exp_f32 v146, v138\n" \
  "v_exp_f32 v147, v139\n" \
  "v_mfma_f32_32x32x16_bf16 v[96:111], v[216:219], v[152:155], v[96:111]\n" \
  "v_cvt_pk_bf16_f32 v156, v144, v145\n" \
  "v_cvt_pk_bf16_f32 v157, v146, v147\n" \
  "v_exp_f32 v148, v140\n" \
  "v_exp_f32 v149, v141\n" \
  "v_exp_f32 v150, v142\n" \
  "v_exp_f32 v151, v143\n" \
  "s_sub_u32 %[cnt], %[cnt], 1\n" \
  "s_cmp_lg_u32 %[cnt], 0\n" \
  "s_cbranch_scc1 1b\n" \
  "v_mfma_f32_32x32x16_bf16 v[128:143], v[160:163], v[24:27], 0\n" \
  "v_cvt_pk_bf16_f32 v158, v148, v149\n" \
  "v_cvt_pk_bf16_f32 v159, v150, v151\n" \
  "v_exp_f32 v144, v112\n" \
  "v_exp_f32 v145, v113\n" \
  "v_exp_f32 v146, v114\n" \
  "v_exp_f32 v147, v115\n" \
  "v_mfma_f32_32x32x16_bf16 v[96:111], v[220:223], v[156:159], v[96:111]\n" \
  "v_cvt_pk_bf16_f32 v152, v144, v145\n" \
  "v_cvt_pk_bf16_f32 v153, v146, v147\n" \
  "v_exp_f32 v148, v116\n" \
  "v_exp_f32 v149, v117\n" \
  "v_exp_f32 v150, v118\n" \
  "v_exp_f32 v151, v119\n" \
  "s_add_u32 %[vo], %[vo], 0x2000\n" \
  "s_and_b32 %[vo], %[vo], 0x1ffff\n" \
  "s_add_u32 s60, s58, %[vo]\n" \
  "s_addc_u32 s61, s59, 0\n" \
  "global_load_dwordx4 v[216:219], %[lo], s[60:61] sc1\n" \
  "global_load_dwordx4 v[220:223], %[lo], s[60:61] offset:1024 sc1\n" \
  "v_mfma_f32_32x32x16_bf16 v[128:143], v[164:167], v[28:31], v[128:143]\n" \
  "v_cvt_pk_bf16_f32 v154, v148, v149\n" \
  "v_cvt_pk_bf16_f32 v155, v150, v151\n" \
  "v_exp_f32 v144, v120\n" \
  "v_exp_f32 v145, v121\n" \
  "v_exp_f32 v146, v122\n" \
  "v_exp_f32 v147, v123\n" \
  "s_waitcnt vmcnt(12)\n" \
  "v_mfma_f32_32x32x16_bf16 v[48:63], v[192:195], v[152:155], v[48:63]\n" \
  "v_cvt_pk_bf16_f32 v156, v144, v145\n" \
  "v_cvt_pk_bf16_f32 v157, v146, v147\n" \
  "v_exp_f32 v148, v124\n" \
  "v_exp_f32 v149, v125\n" \
  "v_exp_f32 v150, v126\n" \
  "v_exp_f32 v151, v127\n" \
  "v_mfma_f32_32x32x16_bf16 v[112:127], v[160:163], v[32:35], 0\n" \
  "v_cvt_pk_bf16_f32 v158, v148, v149\n" \
  "v_cvt_pk_bf16_f32 v159, v150, v151\n" \
  "v_exp_f32 v144, v128\n" \
  "v_exp_f32 v145, v129\n" \
  "v_exp_f32 v146, v130\n" \
  "v_exp_f32 v147, v131\n" \
  "v_mfma_f32_32x32x16_bf16 v[48:63], v[196:199], v[156:159], v[48:63]\n" \
  "v_cvt_pk_bf16_f32 v152, v144, v145\n" \
  "v_cvt_pk_bf16_f32 v153, v146, v147\n" \
  "v_exp_f32 v148, v132\n" \
  "v_exp_f32 v149, v133\n" \
  "v_exp_f32 v150, v134\n" \
  "v_exp_f32 v151, v135\n" \
  "v_mfma_f32_32x32x16_bf16 v[112:127], v[164:167], v[36:39], v[112:127]\n" \
  "v_cvt_pk_bf16_f32 v154, v148, v149\n" \
  "v_cvt_pk_bf16_f32 v155, v150, v151\n" \
  "v_exp_f32 v144, v136\n" \
  "v_exp_f32 v145, v137\n" \
  "v_exp_f32 v146, v138\n" \
  "v_exp_f32 v147, v139\n" \
  "v_mfma_f32_32x32x16_bf16 v[64:79], v[192:195], v[152:155], v[64:79]\n" \
  "v_cvt_pk_bf16_f32 v156, v144, v145\n" \
  "v_cvt_pk_bf16_f32 v157, v146, v147\n" \
  "v_exp_f32 v148, v140\n" \
  "v_exp_f32 v149, v141\n" \
  "v_exp_f32 v150, v142\n" \
  "v_exp_f32 v151, v143\n" \
  "v_mfma_f32_32x32x16_bf16 v[128:143], v[160:163], v[40:43], 0\n" \
  "v_cvt_pk_bf16_f32 v158, v148, v149\n" \
  "v_cvt_pk_bf16_f32 v159, v150, v151\n" \
  "v_exp_f32 v144, v112\n" \
  "v_exp_f32 v145, v113\n" \
  "v_exp_f32 v146, v114\n" \
  "v_exp_f32 v147, v115\n" \
  "v_mfma_f32_32x32x16_bf16 v[64:79], v[196:199], v[156:159], v[64:79]\n" \
  "v_cvt_pk_bf16_f32 v152, v144, v145\n" \
  "v_cvt_pk_bf16_f32 v153, v146, v147\n" \
  "v_exp_f32 v148, v116\n" \
  "v_exp_f32 v149, v117\n" \
  "v_exp_f32 v150, v118\n" \
  "v_exp_f32 v151, v119\n" \
  "v_mfma_f32_32x32x16_bf16 v[128:143], v[164:167], v[44:47], v[128:143]\n" \
  "v_cvt_pk_bf16_f32 v154, v148, v149\n" \
  "v_cvt_pk_bf16_f32 v155, v150, v151\n" \
  "v_exp_f32 v144, v120\n" \
  "v_exp_f32 v145, v121\n" \
  "v_exp_f32 v146, v122\n" \
  "v_exp_f32 v147, v123\n" \
  "v_mfma_f32_32x32x16_bf16 v[80:95], v[192:195], v[152:155], v[80:95]\n" \
  "v_cvt_pk_bf16_f32 v156, v144, v145\n" \
  "v_cvt_pk_bf16_f32 v157, v146, v147\n" \
  "v_exp_f32 v148, v124\n" \
  "v_exp_f32 v149, v125\n" \
  "v_exp_f32 v150, v126\n" \
  "v_exp_f32 v151, v127\n" \
  "s_waitcnt vmcnt(10)\n" \
  "v_mfma_f32_32x32x16_bf16 v[112:127], v[168:171], v[16:19], 0\n" \
  "v_cvt_pk_bf16_f32 v158, v148, v149\n" \
  "v_cvt_pk_bf16_f32 v159, v150, v151\n" \
  "v_exp_f32 v144, v128\n" \
  "v_exp_f32 v145, v129\n" \
  "v_exp_f32 v146, v130\n" \
  "v_exp_f32 v147, v131\n" \
  "v_mfma_f32_32x32x16_bf16 v[80:95], v[196:199], v[156:159], v[80:95]\n" \
  "v_cvt_pk_bf16_f32 v152, v144, v145\n" \
  "v_cvt_pk_bf16_f32 v153, v146, v147\n" \
  "v_exp_f32 v148, v132\n" \
  "v_exp_f32 v149, v133\n" \
  "v_exp_f32 v150, v134\n" \
  "v_exp_f32 v151, v135\n" \
  "v_mfma_f32_32x32x16_bf16 v[112:127], v[172:175], v[20:23], v[112:127]\n" \
  "v_cvt_pk_bf16_f32 v154, v148, v149\n" \
  "v_cvt_pk_bf16_f32 v155, v150, v151\n" \
  "v_exp_f32 v144, v136\n" \
  "v_exp_f32 v145, v137\n" \
  "v_exp_f32 v146, v138\n" \
  "v_exp_f32 v147, v139\n" \
  "v_mfma_f32_32x32x16_bf16 v[96:111], v[192:195], v[152:155], v[96:111]\n" \
  "v_cvt_pk_bf16_f32 v156, v144, v145\n" \
  "v_cvt_pk_bf16_f32 v157, v146, v147\n" \
  "v_exp_f32 v148, v140\n" \
  "v_exp_f32 v149, v141\n" \
  "v_exp_f32 v150, v142\n" \
  "v_exp_f32 v151, v143\n" \
  "v_mfma_f32_32x32x16_bf16 v[128:143], v[168:171], v[24:27], 0\n" \
  "v_cvt_pk_bf16_f32 v158, v148, v149\n" \
  "v_cvt_pk_bf16_f32 v159, v150, v151\n" \
  "v_exp_f32 v144, v112\n" \
  "v_exp_f32 v145, v113\n" \
  "v_exp_f32 v146, v114\n" \
  "v_exp_f32 v147, v115\n" \
  "v_mfma_f32_32x32x16_bf16 v[96:111], v[196:199], v[156:159], v[96:111]\n" \
  "v_cvt_pk_bf16_f32 v152, v144, v145\n" \
  "v_cvt_pk_bf16_f32 v153, v146, v147\n" \
  "v_exp_f32 v148, v116\n" \
  "v_exp_f32 v149, v117\n" \
  "v_exp_f32 v150, v118\n" \
  "v_exp_f32 v151, v119\n" \
  "v_mfma_f32_32x32x16_bf16 v[128:143], v[172:175], v[28:31], v[128:143]\n" \
  "v_cvt_pk_bf16_f32 v154, v148, v149\n" \
  "v_cvt_pk_bf16_f32 v155, v150, v151\n" \
  "v_exp_f32 v144, v120\n" \
  "v_exp_f32 v145, v121\n" \
  "v_exp_f32 v146, v122\n" \
  "v_exp_f32 v147, v123\n" \
  "s_waitcnt vmcnt(8)\n" \
  "v_mfma_f32_32x32x16_bf16 v[48:63], v[200:203], v[152:155], v[48:63]\n" \
  "v_cvt_pk_bf16_f32 v156, v144, v145\n" \
  "v_cvt_pk_bf16_f32 v157, v146, v147\n" \
  "v_exp_f32 v148, v124\n" \
  "v_exp_f32 v149, v125\n" \
  "v_exp_f32 v150, v126\n" \
  "v_exp_f32 v151, v127\n" \
  "v_mfma_f32_32x32x16_bf16 v[112:127], v[168:171], v[32:35], 0\n" \
  "v_cvt_pk_bf16_f32 v158, v148, v149\n" \
  "v_cvt_pk_bf16_f32 v159, v150, v151\n" \
  "v_exp_f32 v144, v128\n" \
  "v_exp_f32 v145, v129\n" \
  "v_exp_f32 v146, v130\n" \
  "v_exp_f32 v147, v131\n" \
  "v_mfma_f32_32x32x16_bf16 v[48:63], v[204:207], v[156:159], v[48:63]\n" \
  "v_cvt_pk_bf16_f32 v152, v144, v145\n" \
  "v_cvt_pk_bf16_f32 v153, v146, v147\n" \
  "v_exp_f32 v148, v132\n" \
  "v_exp_f32 v149, v133\n" \
  "v_exp_f32 v150, v134\n" \
  "v_exp_f32 v151, v135\n" \
  "v_mfma_f32_32x32x16_bf16 v[112:127], v[172:175], v[36:39], v[112:127]\n" \
  "v_cvt_pk_bf16_f32 v154, v148, v149\n" \
  "v_cvt_pk_bf16_f32 v155, v150, v151\n" \
  "v_exp_f32 v144, v136\n" \
  "v_exp_f32 v145, v137\n" \
  "v_exp_f32 v146, v138\n" \
  "v_exp_f32 v147, v139\n" \
  "v_mfma_f32_32x32x16_bf16 v[64:79], v[200:203], v[152:155], v[64:79]\n" \
  "v_cvt_pk_bf16_f32 v156, v144, v145\n" \
  "v_cvt_pk_bf16_f32 v157, v146, v147\n" \
  "v_exp_f32 v148, v140\n" \
  "v_exp_f32 v149, v141\n" \
  "v_exp_f32 v150, v142\n" \
  "v_exp_f32 v151, v143\n" \
  "v_mfma_f32_32x32x16_bf16 v[128:143], v[168:171], v[40:43], 0\n" \
  "v_cvt_pk_bf16_f32 v158, v148, v149\n" \
  "v_cvt_pk_bf16_f32 v159, v150, v151\n" \
  "v_exp_f32 v144, v112\n" \
  "v_exp_f32 v145, v113\n" \
  "v_exp_f32 v146, v114\n" \
  "v_exp_f32 v147, v115\n" \
  "v_mfma_f32_32x32x16_bf16 v[64:79], v[204:207], v[156:159], v[64:79]\n" \
  "v_cvt_pk_bf16_f32 v152, v144, v145\n" \
  "v_cvt_pk_bf16_f32 v153, v146, v147\n" \
  "v_exp_f32 v148, v116\n" \
  "v_exp_f32 v149, v117\n" \
  "v_exp_f32 v150, v118\n" \
  "v_exp_f32 v151, v119\n" \
  "v_mfma_f32_32x32x16_bf16 v[128:143], v[172:175], v[44:47], v[128:143]\n" \
  "v_cvt_pk_bf16_f32 v154, v148, v149\n" \
  "v_cvt_pk_bf16_f32 v155, v150, v151\n" \
  "v_exp_f32 v144, v120\n" \
  "v_exp_f32 v145, v121\n" \
  "v_exp_f32 v146, v122\n" \
  "v_exp_f32 v147, v123\n" \
  "v_mfma_f32_32x32x16_bf16 v[80:95], v[200:203], v[152:155], v[80:95]\n" \
  "v_cvt_pk_bf16_f32 v156, v144, v145\n" \
  "v_cvt_pk_bf16_f32 v157, v146, v147\n" \
  "v_exp_f32 v148, v124\n" \
  "v_exp_f32 v149, v125\n" \
  "v_exp_f32 v150, v126\n" \
  "v_exp_f32 v151, v127\n" \
  "s_waitcnt vmcnt(6)\n" \
  "v_mfma_f32_32x32x16_bf16 v[112:127], v[176:179], v[16:19], 0\n" \
  "v_cvt_pk_bf16_f32 v158, v148, v149\n" \
  "v_cvt_pk_bf16_f32 v159, v150, v151\n" \
  "v_exp_f32 v144, v128\n" \
  "v_exp_f32 v145, v129\n" \
  "v_exp_f32 v146, v130\n" \
  "v_exp_f32 v147, v131\n" \
  "v_mfma_f32_32x32x16_bf16 v[80:95], v[204:207], v[156:159], v[80:95]\n" \
  "v_cvt_pk_bf16_f32 v152, v144, v145\n" \
  "v_cvt_pk_bf16_f32 v153, v146, v147\n" \
  "v_exp_f32 v148, v132\n" \
  "v_exp_f32 v149, v133\n" \
  "v_exp_f32 v150, v134\n" \
  "v_exp_f32 v151, v135\n" \
  "v_mfma_f32_32x32x16_bf16 v[112:127], v[180:183], v[20:23], v[112:127]\n" \
  "v_cvt_pk_bf16_f32 v154, v148, v149\n" \
  "v_cvt_pk_bf16_f32 v155, v150, v151\n" \
  "v_exp_f32 v144, v136\n" \
  "v_exp_f32 v145, v137\n" \
  "v_exp_f32 v146, v138\n" \
  "v_exp_f32 v147, v139\n" \
  "v_mfma_f32_32x32x16_bf16 v[96:111], v[200:203], v[152:155], v[96:111]\n" \
  "v_cvt_pk_bf16_f32 v156, v144, v145\n" \
  "v_cvt_pk_bf16_f32 v157, v146, v147\n" \
  "v_exp_f32 v148, v140\n" \
  "v_exp_f32 v149, v141\n" \
  "v_exp_f32 v150, v142\n" \
  "v_exp_f32 v151, v143\n" \
  "v_mfma_f32_32x32x16_bf16 v[128:143], v[176:179], v[24:27], 0\n" \
  "v_cvt_pk_bf16_f32 v158, v148, v149\n" \
  "v_cvt_pk_bf16_f32 v159, v150, v151\n" \
  "v_exp_f32 v144, v112\n" \
  "v_exp_f32 v145, v113\n" \
  "v_exp_f32 v146, v114\n" \
  "v_exp_f32 v147, v115\n" \
  "v_mfma_f32_32x32x16_bf16 v[96:111], v[204:207], v[156:159], v[96:111]\n" \
  "v_cvt_pk_bf16_f32 v152, v144, v145\n" \
  "v_cvt_pk_bf16_f32 v153, v146, v147\n" \
  "v_exp_f32 v148, v116\n" \
  "v_exp_f32 v149, v117\n" \
  "v_exp_f32 v150, v118\n" \
  "v_exp_f32 v151, v119\n" \
  "v_mfma_f32_32x32x16_bf16 v[128:143], v[180:183], v[28:31], v[128:143]\n" \
  "v_cvt_pk_bf16_f32 v154, v148, v149\n" \
  "v_cvt_pk_bf16_f32 v155, v150, v151\n" \
  "v_exp_f32 v144, v120\n" \
  "v_exp_f32 v145, v121\n" \
  "v_exp_f32 v146, v122\n" \
  "v_exp_f32 v147, v123\n" \
  "s_waitcnt vmcnt(4)\n" \
  "v_mfma_f32_32x32x16_bf16 v[48:63], v[208:211], v[152:155], v[48:63]\n" \
  "v_cvt_pk_bf16_f32 v156, v144, v145\n" \
  "v_cvt_pk_bf16_f32 v157, v146, v147\n" \
  "v_exp_f32 v148, v124\n" \
  "v_exp_f32 v149, v125\n" \
  "v_exp_f32 v150, v126\n" \
  "v_exp_f32 v151, v127\n" \
  "v_mfma_f32_32x32x16_bf16 v[112:127], v[176:179], v[32:35], 0\n" \
  "v_cvt_pk_bf16_f32 v158, v148, v149\n" \
  "v_cvt_pk_bf16_f32 v159, v150, v151\n" \
  "v_exp_f32 v144, v128\n" \
  "v_exp_f32 v145, v129\n" \
  "v_exp_f32 v146, v130\n" \
  "v_exp_f32 v147, v131\n" \
  "v_mfma_f32_32x32x16_bf16 v[48:63], v[212:215], v[156:159], v[48:63]\n" \
  "v_cvt_pk_bf16_f32 v152, v144, v145\n" \
  "v_cvt_pk_bf16_f32 v153, v146, v147\n" \
  "v_exp_f32 v148, v132\n" \
  "v_exp_f32 v149, v133\n" \
  "v_exp_f32 v150, v134\n" \
  "v_exp_f32 v151, v135\n" \
  "v_mfma_f32_32x32x16_bf16 v[112:127], v[180:183], v[36:39], v[112:127]\n" \
  "v_cvt_pk_bf16_f32 v154, v148, v149\n" \
  "v_cvt_pk_bf16_f32 v155, v150, v151\n" \
  "v_exp_f32 v144, v136\n" \
  "v_exp_f32 v145, v137\n" \
  "v_exp_f32 v146, v138\n" \
  "v_exp_f32 v147, v139\n" \
  "v_mfma_f32_32x32x16_bf16 v[64:79], v[208:211], v[152:155], v[64:79]\n" \
  "v_cvt_pk_bf16_f32 v156, v144, v145\n" \
  "v_cvt_pk_bf16_f32 v157, v146, v147\n" \
  "v_exp_f32 v148, v140\n" \
  "v_exp_f32 v149, v141\n" \
  "v_exp_f32 v150, v142\n" \
  "v_exp_f32 v151, v143\n" \
  "v_mfma_f32_32x32x16_bf16 v[128:143], v[176:179], v[40:43], 0\n" \
  "v_cvt_pk_bf16_f32 v158, v148, v149\n" \
  "v_cvt_pk_bf16_f32 v159, v150, v151\n" \
  "v_exp_f32 v144, v112\n" \
  "v_exp_f32 v145, v113\n" \
  "v_exp_f32 v146, v114\n" \
  "v_exp_f32 v147, v115\n" \
  "v_mfma_f32_32x32x16_bf16 v[64:79], v[212:215], v[156:159], v[64:79]\n" \
  "v_cvt_pk_bf16_f32 v152, v144, v145\n" \
  "v_cvt_pk_bf16_f32 v153, v146, v147\n" \
  "v_exp_f32 v148, v116\n" \
  "v_exp_f32 v149, v117\n" \
  "v_exp_f32 v150, v118\n" \
  "v_exp_f32 v151, v119\n" \
  "v_mfma_f32_32x32x16_bf16 v[128:143], v[180:183], v[44:47], v[128:143]\n" \
  "v_cvt_pk_bf16_f32 v154, v148, v149\n" \
  "v_cvt_pk_bf16_f32 v155, v150, v151\n" \
  "v_exp_f32 v144, v120\n" \
  "v_exp_f32 v145, v121\n" \
  "v_exp_f32 v146, v122\n" \
  "v_exp_f32 v147, v123\n" \
  "v_mfma_f32_32x32x16_bf16 v[80:95], v[208:211], v[152:155], v[80:95]\n" \
  "v_cvt_pk_bf16_f32 v156, v144, v145\n" \
  "v_cvt_pk_bf16_f32 v157, v146, v147\n" \
  "v_exp_f32 v148, v124\n" \
  "v_exp_f32 v149, v125\n" \
  "v_exp_f32 v150, v126\n" \
  "v_exp_f32 v151, v127\n" \
  "s_waitcnt vmcnt(2)\n" \
  "v_mfma_f32_32x32x16_bf16 v[112:127], v[184:187], v[16:19], 0\n" \
  "v_cvt_pk_bf16_f32 v158, v148, v149\n" \
  "v_cvt_pk_bf16_f32 v159, v150, v151\n" \
  "v_exp_f32 v144, v128\n" \
  "v_exp_f32 v145, v129\n" \
  "v_exp_f32 v146, v130\n" \
  "v_exp_f32 v147, v131\n" \
  "v_mfma_f32_32x32x16_bf16 v[80:95], v[212:215], v[156:159], v[80:95]\n" \
  "v_cvt_pk_bf16_f32 v152, v144, v145\n" \
  "v_cvt_pk_bf16_f32 v153, v146, v147\n" \
  "v_exp_f32 v148, v132\n" \
  "v_exp_f32 v149, v133\n" \
  "v_exp_f32 v150, v134\n" \
  "v_exp_f32 v151, v135\n" \
  "v_mfma_f32_32x32x16_bf16 v[112:127], v[188:191], v[20:23], v[112:127]\n" \
  "v_cvt_pk_bf16_f32 v154, v148, v149\n" \
  "v_cvt_pk_bf16_f32 v155, v150, v151\n" \
  "v_exp_f32 v144, v136\n" \
  "v_exp_f32 v145, v137\n" \
  "v_exp_f32 v146, v138\n" \
  "v_exp_f32 v147, v139\n" \
  "v_mfma_f32_32x32x16_bf16 v[96:111], v[208:211], v[152:155], v[96:111]\n" \
  "v_cvt_pk_bf16_f32 v156, v144, v145\n" \
  "v_cvt_pk_bf16_f32 v157, v146, v147\n" \
  "v_exp_f32 v148, v140\n" \
  "v_exp_f32 v149, v141\n" \
  "v_exp_f32 v150, v142\n" \
  "v_exp_f32 v151, v143\n" \
  "v_mfma_f32_32x32x16_bf16 v[128:143], v[184:187], v[24:27], 0\n" \
  "v_cvt_pk_bf16_f32 v158, v148, v149\n" \
  "v_cvt_pk_bf16_f32 v159, v150, v151\n" \
  "v_exp_f32 v144, v112\n" \
  "v_exp_f32 v145, v113\n" \
  "v_exp_f32 v146, v114\n" \
  "v_exp_f32 v147, v115\n" \
  "v_mfma_f32_32x32x16_bf16 v[96:111], v[212:215], v[156:159], v[96:111]\n" \
  "v_cvt_pk_bf16_f32 v152, v144, v145\n" \
  "v_cvt_pk_bf16_f32 v153, v146, v147\n" \
  "v_exp_f32 v148, v116\n" \
  "v_exp_f32 v149, v117\n" \
  "v_exp_f32 v150, v118\n" \
  "v_exp_f32 v151, v119\n" \
  "v_mfma_f32_32x32x16_bf16 v[128:143], v[188:191], v[28:31], v[128:143]\n" \
  "v_cvt_pk_bf16_f32 v154, v148, v149\n" \
  "v_cvt_pk_bf16_f32 v155, v150, v151\n" \
  "v_exp_f32 v144, v120\n" \
  "v_exp_f32 v145, v121\n" \
  "v_exp_f32 v146, v122\n" \
  "v_exp_f32 v147, v123\n" \
  "s_waitcnt vmcnt(0)\n" \
  "v_mfma_f32_32x32x16_bf16 v[48:63], v[216:219], v[152:155], v[48:63]\n" \
  "v_cvt_pk_bf16_f32 v156, v144, v145\n" \
  "v_cvt_pk_bf16_f32 v157, v146, v147\n" \
  "v_exp_f32 v148, v124\n" \
  "v_exp_f32 v149, v125\n" \
  "v_exp_f32 v150, v126\n" \
  "v_exp_f32 v151, v127\n" \
  "v_mfma_f32_32x32x16_bf16 v[112:127], v[184:187], v[32:35], 0\n" \
  "v_cvt_pk_bf16_f32 v158, v148, v149\n" \
  "v_cvt_pk_bf16_f32 v159, v150, v151\n" \
  "v_exp_f32 v144, v128\n" \
  "v_exp_f32 v145, v129\n" \
  "v_exp_f32 v146, v130\n" \
  "v_exp_f32 v147, v131\n" \
  "v_mfma_f32_32x32x16_bf16 v[48:63], v[220:223], v[156:159], v[48:63]\n" \
  "v_cvt_pk_bf16_f32 v152, v144, v145\n" \
  "v_cvt_pk_bf16_f32 v153, v146, v147\n" \
  "v_exp_f32 v148, v132\n" \
  "v_exp_f32 v149, v133\n" \
  "v_exp_f32 v150, v134\n" \
  "v_exp_f32 v151, v135\n" \
  "v_mfma_f32_32x32x16_bf16 v[112:127], v[188:191], v[36:39], v[112:127]\n" \
  "v_cvt_pk_bf16_f32 v154, v148, v149\n" \
  "v_cvt_pk_bf16_f32 v155, v150, v151\n" \
  "v_exp_f32 v144, v136\n" \
  "v_exp_f32 v145, v137\n" \
  "v_exp_f32 v146, v138\n" \
  "v_exp_f32 v147, v139\n" \
  "v_mfma_f32_32x32x16_bf16 v[64:79], v[216:219], v[152:155], v[64:79]\n" \
  "v_cvt_pk_bf16_f32 v156, v144, v145\n" \
  "v_cvt_pk_bf16_f32 v157, v146, v147\n" \
  "v_exp_f32 v148, v140\n" \
  "v_exp_f32 v149, v141\n" \
  "v_exp_f32 v150, v142\n" \
  "v_exp_f32 v151, v143\n" \
  "v_mfma_f32_32x32x16_bf16 v[128:143], v[184:187], v[40:43], 0\n" \
  "v_cvt_pk_bf16_f32 v158, v148, v149\n" \
  "v_cvt_pk_bf16_f32 v159, v150, v151\n" \
  "v_exp_f32 v144, v112\n" \
  "v_exp_f32 v145, v113\n" \
  "v_exp_f32 v146, v114\n" \
  "v_exp_f32 v147, v115\n" \
  "ds_write_b128 %[r0], v[48:51] offset:0\n" \
  "v_mfma_f32_32x32x16_bf16 v[64:79], v[220:223], v[156:159], v[64:79]\n" \
  "v_cvt_pk_bf16_f32 v152, v144, v145\n" \
  "v_cvt_pk_bf16_f32 v153, v146, v147\n" \
  "v_exp_f32 v148, v116\n" \
  "v_exp_f32 v149, v117\n" \
  "v_exp_f32 v150, v118\n" \
  "v_exp_f32 v151, v119\n" \
  "ds_write_b128 %[r0], v[52:55] offset:1024\n" \
  "v_mfma_f32_32x32x16_bf16 v[128:143], v[188:191], v[44:47], v[128:143]\n" \
  "v_cvt_pk_bf16_f32 v154, v148, v149\n" \
  "v_cvt_pk_bf16_f32 v155, v150, v151\n" \
  "v_exp_f32 v144, v120\n" \
  "v_exp_f32 v145, v121\n" \
  "v_exp_f32 v146, v122\n" \
  "v_exp_f32 v147, v123\n" \
  "ds_write_b128 %[r0], v[56:59] offset:2048\n" \
  "v_mfma_f32_32x32x16_bf16 v[80:95], v[216:219], v[152:155], v[80:95]\n" \
  "v_cvt_pk_bf16_f32 v156, v144, v145\n" \
  "v_cvt_pk_bf16_f32 v157, v146, v147\n" \
  "v_exp_f32 v148, v124\n" \
  "v_exp_f32 v149, v125\n" \
  "v_exp_f32 v150, v126\n" \
  "v_exp_f32 v151, v127\n" \
  "ds_write_b128 %[r0], v[60:63] offset:3072\n" \
  "v_cvt_pk_bf16_f32 v158, v148, v149\n" \
  "v_cvt_pk_bf16_f32 v159, v150, v151\n" \
  "v_exp_f32 v144, v128\n" \
  "v_exp_f32 v145, v129\n" \
  "v_exp_f32 v146, v130\n" \
  "v_exp_f32 v147, v131\n" \
  "ds_write_b128 %[r1], v[64:67] offset:0\n" \
  "v_mfma_f32_32x32x16_bf16 v[80:95], v[220:223], v[156:159], v[80:95]\n" \
  "v_cvt_pk_bf16_f32 v152, v144, v145\n" \
  "v_cvt_pk_bf16_f32 v153, v146, v147\n" \
  "v_exp_f32 v148, v132\n" \
  "v_exp_f32 v149, v133\n" \
  "v_exp_f32 v150, v134\n" \
  "v_exp_f32 v151, v135\n" \
  "ds_write_b128 %[r1], v[68:71] offset:1024\n" \
  "v_cvt_pk_bf16_f32 v154, v148, v149\n" \
  "v_cvt_pk_bf16_f32 v155, v150, v151\n" \
  "v_exp_f32 v144, v136\n" \
  "v_exp_f32 v145, v137\n" \
  "v_exp_f32 v146, v138\n" \
  "v_exp_f32 v147, v139\n" \
  "ds_write_b128 %[r1], v[72:75] offset:2048\n" \
  "v_mfma_f32_32x32x16_bf16 v[96:111], v[216:219], v[152:155], v[96:111]\n" \
  "v_cvt_pk_bf16_f32 v156, v144, v145\n" \
  "v_cvt_pk_bf16_f32 v157, v146, v147\n" \
  "v_exp_f32 v148, v140\n" \
  "v_exp_f32 v149, v141\n" \
  "v_exp_f32 v150, v142\n" \
  "v_exp_f32 v151, v143\n" \
  "ds_write_b128 %[r1], v[76:79] offset:3072\n" \
  "v_cvt_pk_bf16_f32 v158, v148, v149\n" \
  "v_cvt_pk_bf16_f32 v159, v150, v151\n" \
  "ds_write_b128 %[r2], v[80:83] offset:0\n" \
  "ds_write_b128 %[r2], v[84:87] offset:1024\n" \
  "v_mfma_f32_32x32x16_bf16 v[96:111], v[220:223], v[156:159], v[96:111]\n" \
  "ds_write_b128 %[r2], v[88:91] offset:2048\n" \
  "ds_write_b128 %[r2], v[92:95] offset:3072\n" \
  "s_nop 15\n"
#define ATTN_LOOP_CLOBBERS "v16", "v17", "v18", "v19", "v20", "v21", "v22", "v23", "v24", "v25", "v26", "v27", "v28", "v29", "v30", "v31", "v32", "v33", "v34", "v35", "v36", "v37", "v38", "v39", "v48", "v49", "v50", "v51", "v52", "v53", "v54", "v55", "v56", "v57", "v58", "v59", "v60", "v61", "v62", "v63", "v64", "v65", "v66", "v67", "v68", "v69", "v70", "v71", "v72", "v73", "v74", "v75", "v76", "v77", "v78", "v79", "v80", "v81", "v82", "v83", "v84", "v85", "v86", "v87", "v88", "v89", "v90", "v91", "v92", "v93", "v94", "v95", "v112", "v113", "v114", "v115", "v116", "v117", "v118", "v119", "v120", "v121", "v122", "v123", "v124", "v125", "v126", "v127", "v128", "v129", "v130", "v131", "v132", "v133", "v134", "v135", "v136", "v137", "v138", "v139", "v140", "v141", "v142", "v143", "v144", "v145", "v146", "v147", "v148", "v149", "v150", "v151", "v152", "v153", "v154", "v155", "v156", "v157", "v158", "v159", "v168", "v169", "v170", "v171", "v172", "v173", "v174", "v175", "v176", "v177", "v178", "v179", "v180", "v181", "v182", "v183", "v184", "v185", "v186", "v187", "v188", "v189", "v190", "v191", "v200", "v201", "v202", "v203", "v204", "v205", "v206", "v207", "v208", "v209", "v210", "v211", "v212", "v213", "v214", "v215", "v216", "v217", "v218", "v219", "v220", "v221", "v222", "v223", "v224", "v225", "v226", "v227", "v228", "v229", "v230", "v231", "v232", "v233", "v234", "v235", "v236", "v237", "v238", "v239", "v240", "v241", "v242", "v243", "v244", "v245", "v246", "v247", "s56", "s57", "s58", "s59", "s60", "s61", "s62", "s63", "v249", "vcc", "scc", "memory"

#define QKV_CLOBBERS "v64", "v65", "v66", "v67", "v68", "v69", "v70", "v71", "v72", "v73", "v74", "v75", "v76", "v77", "v78", "v79", "v80", "v81", "v82", "v83", "v84", "v85", "v86", "v87", "v88", "v89", "v90", "v91", "v92", "v93", "v94", "v95", "v96", "v97", "v98", "v99", "v100", "v101", "v102", "v103", "v104", "v105", "v106", "v107", "v108", "v109", "v110", "v111", "v112", "v113", "v114", "v115", "v116", "v117", "v118", "v119", "v120", "v121", "v122", "v123", "v124", "v125", "v126", "v127", "v128", "v129", "v130", "v131", "v132", "v133", "v134", "v135", "v136", "v137", "v138", "v139", "v140", "v141", "v142", "v143", "v144", "v145", "v146", "v147", "v148", "v149", "v150", "v151", "v152", "v153", "v154", "v155", "v156", "v157", "v158", "v159", "v160", "v161", "v162", "v163", "v164", "v165", "v166", "v167", "v168", "v169", "v170", "v171", "v172", "v173", "v174", "v175", "v176", "v177", "v178", "v179", "v180", "v181", "v182", "v183", "v184", "v185", "v186", "v187", "v188", "v189", "v190", "v191", "v192", "v193", "v194", "v195", "v196", "v197", "v198", "v199", "v200", "v201", "v202", "v203", "v204", "v205", "v206", "v207", "s56", "s57", "s58", "s59", "scc", "memory"
#define QKV_ASM_QKV \
  "s_mov_b32 s57, 0\n" \
  "v_add_u32 v192, 0, %[x0]\n" \
  "v_add_u32 v193, 0, %[x1]\n" \
  "v_add_u32 v194, 0, %[x2]\n" \
  "v_add_u32 v195, 0, %[x3]\n" \
  "v_add_u32 v196, 0, %[w0]\n" \
  "v_add_u32 v197, 0, %[w1]\n" \
  "v_add_u32 v198, 0, %[w2]\n" \
  "L0_%=:\n" \
  "s_sleep 2\n" \
  "ds_read_b128 v[204:207], %[ct]\n" \
  "s_waitcnt lgkmcnt(0)\n" \
  "v_min_u32 v204, v204, v205\n" \
  "v_min3_u32 v204, v204, v206, v207\n" \
  "s_nop 0\n" \
  "v_readfirstlane_b32 s56, v204\n" \
  "s_add_u32 s57, s57, 1\n" \
  "s_bitcmp1_b32 s57, 18\n" \
  "s_cbranch_scc1 G0_%=\n" \
  "s_cmp_lt_u32 s56, 1\n" \
  "s_cbranch_scc1 L0_%=\n" \
  "G0_%=:\n" \
  "ds_read_b128 v[64:67], v192\n" \
  "ds_read_b64_tr_b16 v[68:69], v196 offset:0\n" \
  "ds_read_b64_tr_b16 v[70:71], v196 offset:256\n" \
  "ds_read_b64_tr_b16 v[72:73], v197 offset:0\n" \
  "ds_read_b64_tr_b16 v[74:75], v197 offset:256\n" \
  "ds_read_b64_tr_b16 v[76:77], v198 offset:0\n" \
  "ds_read_b64_tr_b16 v[78:79], v198 offset:256\n" \
  "ds_read_b128 v[80:83], v193\n" \
  "ds_read_b64_tr_b16 v[84:85], v196 offset:1024\n" \
  "ds_read_b64_tr_b16 v[86:87], v196 offset:1280\n" \
  "ds_read_b64_tr_b16 v[88:89], v197 offset:1024\n" \
  "ds_read_b64_tr_b16 v[90:91], v197 offset:1280\n" \
  "ds_read_b64_tr_b16 v[92:93], v198 offset:1024\n" \
  "ds_read_b64_tr_b16 v[94:95], v198 offset:1280\n" \
  "ds_read_b128 v[96:99], v194\n" \
  "ds_read_b64_tr_b16 v[100:101], v196 offset:2048\n" \
  "ds_read_b64_tr_b16 v[102:103], v196 offset:2304\n" \
  "ds_read_b64_tr_b16 v[104:105], v197 offset:2048\n" \
  "ds_read_b64_tr_b16 v[106:107], v197 offset:2304\n" \
  "ds_read_b64_tr_b16 v[108:109], v198 offset:2048\n" \
  "ds_read_b64_tr_b16 v[110:111], v198 offset:2304\n" \
  "s_waitcnt lgkmcnt(14)\n" \
  "v_mfma_f32_32x32x16_bf16 v[16:31], v[68:71], v[64:67], 0\n" \
  "v_mfma_f32_32x32x16_bf16 v[32:47], v[72:75], v[64:67], 0\n" \
  "v_mfma_f32_32x32x16_bf16 v[48:63], v[64:67], v[76:79], 0\n" \
  "ds_read_b128 v[204:207], %[ct]\n" \
  "ds_read_b128 v[112:115], v195\n" \
  "ds_read_b64_tr_b16 v[116:117], v196 offset:3072\n" \
  "ds_read_b64_tr_b16 v[118:119], v196 offset:3328\n" \
  "ds_read_b64_tr_b16 v[120:121], v197 offset:3072\n" \
  "ds_read_b64_tr_b16 v[122:123], v197 offset:3328\n" \
  "ds_read_b64_tr_b16 v[124:125], v198 offset:3072\n" \
  "ds_read_b64_tr_b16 v[126:127], v198 offset:3328\n" \
  "s_waitcnt lgkmcnt(14)\n" \
  "v_mfma_f32_32x32x16_bf16 v[16:31], v[84:87], v[80:83], v[16:31]\n" \
  "v_mfma_f32_32x32x16_bf16 v[32:47], v[88:91], v[80:83], v[32:47]\n" \
  "v_mfma_f32_32x32x16_bf16 v[48:63], v[80:83], v[92:95], v[48:63]\n" \
  "v_add_u32 v192, 28672, %[x0]\n" \
  "v_add_u32 v193, 28672, %[x1]\n" \
  "v_add_u32 v194, 28672, %[x2]\n" \
  "v_add_u32 v195, 28672, %[x3]\n" \
  "v_add_u32 v196, 28672, %[w0]\n" \
  "v_add_u32 v197, 28672, %[w1]\n" \
  "v_add_u32 v198, 28672, %[w2]\n" \
  "s_waitcnt lgkmcnt(7)\n" \
  "v_min_u32 v204, v204, v205\n" \
  "v_min3_u32 v204, v204, v206, v207\n" \
  "s_nop 0\n" \
  "v_readfirstlane_b32 s56, v204\n" \
  "s_cmp_ge_u32 s56, 2\n" \
  "s_cbranch_scc1 G1_%=\n" \
  "L1_%=:\n" \
  "s_sleep 2\n" \
  "ds_read_b128 v[204:207], %[ct]\n" \
  "s_waitcnt lgkmcnt(0)\n" \
  "v_min_u32 v204, v204, v205\n" \
  "v_min3_u32 v204, v204, v206, v207\n" \
  "s_nop 0\n" \
  "v_readfirstlane_b32 s56, v204\n" \
  "s_add_u32 s57, s57, 1\n" \
  "s_bitcmp1_b32 s57, 18\n" \
  "s_cbranch_scc1 G1_%=\n" \
  "s_cmp_lt_u32 s56, 2\n" \
  "s_cbranch_scc1 L1_%=\n" \
  "G1_%=:\n" \
  "ds_read_b128 v[128:131], v192\n" \
  "ds_read_b64_tr_b16 v[132:133], v196 offset:0\n" \
  "ds_read_b64_tr_b16 v[134:135], v196 offset:256\n" \
  "ds_read_b64_tr_b16 v[136:137], v197 offset:0\n" \
  "ds_read_b64_tr_b16 v[138:139], v197 offset:256\n" \
  "ds_read_b64_tr_b16 v[140:141], v198 offset:0\n" \
  "ds_read_b64_tr_b16 v[142:143], v198 offset:256\n" \
  "s_waitcnt lgkmcnt(14)\n" \
  "v_mfma_f32_32x32x16_bf16 v[16:31], v[100:103], v[96:99], v[16:31]\n" \
  "v_mfma_f32_32x32x16_bf16 v[32:47], v[104:107], v[96:99], v[32:47]\n" \
  "v_mfma_f32_32x32x16_bf16 v[48:63], v[96:99], v[108:111], v[48:63]\n" \
  "ds_read_b128 v[144:147], v193\n" \
  "ds_read_b64_tr_b16 v[148:149], v196 offset:1024\n" \
  "ds_read_b64_tr_b16 v[150:151], v196 offset:1280\n" \
  "ds_read_b64_tr_b16 v[152:153], v197 offset:1024\n" \
  "ds_read_b64_tr_b16 v[154:155], v197 offset:1280\n" \
  "ds_read_b64_tr_b16 v[156:157], v198 offset:1024\n" \
  "ds_read_b64_tr_b16 v[158:159], v198 offset:1280\n" \
  "s_waitcnt lgkmcnt(14)\n" \
  "v_mfma_f32_32x32x16_bf16 v[16:31], v[116:119], v[112:115], v[16:31]\n" \
  "v_mfma_f32_32x32x16_bf16 v[32:47], v[120:123], v[112:115], v[32:47]\n" \
  "v_mfma_f32_32x32x16_bf16 v[48:63], v[112:115], v[124:127], v[48:63]\n" \
  "v_mov_b32 v200, 1\n" \
  "ds_write_b32 %[fr], v200\n" \
  "ds_read_b128 v[160:163], v194\n" \
  "ds_read_b64_tr_b16 v[164:165], v196 offset:2048\n" \
  "ds_read_b64_tr_b16 v[166:167], v196 offset:2304\n" \
  "ds_read_b64_tr_b16 v[168:169], v197 offset:2048\n" \
  "ds_read_b64_tr_b16 v[170:171], v197 offset:2304\n" \
  "ds_read_b64_tr_b16 v[172:173], v198 offset:2048\n" \
  "ds_read_b64_tr_b16 v[174:175], v198 offset:2304\n" \
  "s_waitcnt lgkmcnt(14)\n" \
  "v_mfma_f32_32x32x16_bf16 v[16:31], v[132:135], v[128:131], v[16:31]\n" \
  "v_mfma_f32_32x32x16_bf16 v[32:47], v[136:139], v[128:131], v[32:47]\n" \
  "v_mfma_f32_32x32x16_bf16 v[48:63], v[128:131], v[140:143], v[48:63]\n" \
  "ds_read_b128 v[204:207], %[ct]\n" \
  "ds_read_b128 v[176:179], v195\n" \
  "ds_read_b64_tr_b16 v[180:181], v196 offset:3072\n" \
  "ds_read_b64_tr_b16 v[182:183], v196 offset:3328\n" \
  "ds_read_b64_tr_b16 v[184:185], v197 offset:3072\n" \
  "ds_read_b64_tr_b16 v[186:187], v197 offset:3328\n" \
  "ds_read_b64_tr_b16 v[188:189], v198 offset:3072\n" \
  "ds_read_b64_tr_b16 v[190:191], v198 offset:3328\n" \
  "s_waitcnt lgkmcnt(14)\n" \
  "v_mfma_f32_32x32x16_bf16 v[16:31], v[148:151], v[144:147], v[16:31]\n" \
  "v_mfma_f32_32x32x16_bf16 v[32:47], v[152:155], v[144:147], v[32:47]\n" \
  "v_mfma_f32_32x32x16_bf16 v[48:63], v[144:147], v[156:159], v[48:63]\n" \
  "v_add_u32 v192, 57344, %[x0]\n" \
  "v_add_u32 v193, 57344, %[x1]\n" \
  "v_add_u32 v194, 57344, %[x2]\n" \
  "v_add_u32 v195, 57344, %[x3]\n" \
  "v_add_u32 v196, 57344, %[w0]\n" \
  "v_add_u32 v197, 57344, %[w1]\n" \
  "v_add_u32 v198, 57344, %[w2]\n" \
  "s_waitcnt lgkmcnt(7)\n" \
  "v_min_u32 v204, v204, v205\n" \
  "v_min3_u32 v204, v204, v206, v207\n" \
  "s_nop 0\n" \
  "v_readfirstlane_b32 s56, v204\n" \
  "s_cmp_ge_u32 s56, 3\n" \
  "s_cbranch_scc1 G2_%=\n" \
  "L2_%=:\n" \
  "s_sleep 2\n" \
  "ds_read_b128 v[204:207], %[ct]\n" \
  "s_waitcnt lgkmcnt(0)\n" \
  "v_min_u32 v204, v204, v205\n" \
  "v_min3_u32 v204, v204, v206, v207\n" \
  "s_nop 0\n" \
  "v_readfirstlane_b32 s56, v204\n" \
  "s_add_u32 s57, s57, 1\n" \
  "s_bitcmp1_b32 s57, 18\n" \
  "s_cbranch_scc1 G2_%=\n" \
  "s_cmp_lt_u32 s56, 3\n" \
  "s_cbranch_scc1 L2_%=\n" \
  "G2_%=:\n" \
  "ds_read_b128 v[64:67], v192\n" \
  "ds_read_b64_tr_b16 v[68:69], v196 offset:0\n" \
  "ds_read_b64_tr_b16 v[70:71], v196 offset:256\n" \
  "ds_read_b64_tr_b16 v[72:73], v197 offset:0\n" \
  "ds_read_b64_tr_b16 v[74:75], v197 offset:256\n" \
  "ds_read_b64_tr_b16 v[76:77], v198 offset:0\n" \
  "ds_read_b64_tr_b16 v[78:79], v198 offset:256\n" \
  "s_waitcnt lgkmcnt(14)\n" \
  "v_mfma_f32_32x32x16_bf16 v[16:31], v[164:167], v[160:163], v[16:31]\n" \
  "v_mfma_f32_32x32x16_bf16 v[32:47], v[168:171], v[160:163], v[32:47]\n" \
  "v_mfma_f32_32x32x16_bf16 v[48:63], v[160:163], v[172:175], v[48:63]\n" \
  "ds_read_b128 v[80:83], v193\n" \
  "ds_read_b64_tr_b16 v[84:85], v196 offset:1024\n" \
  "ds_read_b64_tr_b16 v[86:87], v196 offset:1280\n" \
  "ds_read_b64_tr_b16 v[88:89], v197 offset:1024\n" \
  "ds_read_b64_tr_b16 v[90:91], v197 offset:1280\n" \
  "ds_read_b64_tr_b16 v[92:93], v198 offset:1024\n" \
  "ds_read_b64_tr_b16 v[94:95], v198 offset:1280\n" \
  "s_waitcnt lgkmcnt(14)\n" \
  "v_mfma_f32_32x32x16_bf16 v[16:31], v[180:183], v[176:179], v[16:31]\n" \
  "v_mfma_f32_32x32x16_bf16 v[32:47], v[184:187], v[176:179], v[32:47]\n" \
  "v_mfma_f32_32x32x16_bf16 v[48:63], v[176:179], v[188:191], v[48:63]\n" \
  "v_mov_b32 v200, 2\n" \
  "ds_write_b32 %[fr], v200\n" \
  "ds_read_b128 v[96:99], v194\n" \
  "ds_read_b64_tr_b16 v[100:101], v196 offset:2048\n" \
  "ds_read_b64_tr_b16 v[102:103], v196 offset:2304\n" \
  "ds_read_b64_tr_b16 v[104:105], v197 offset:2048\n" \
  "ds_read_b64_tr_b16 v[106:107], v197 offset:2304\n" \
  "ds_read_b64_tr_b16 v[108:109], v198 offset:2048\n" \
  "ds_read_b64_tr_b16 v[110:111], v198 offset:2304\n" \
  "s_waitcnt lgkmcnt(14)\n" \
  "v_mfma_f32_32x32x16_bf16 v[16:31], v[68:71], v[64:67], v[16:31]\n" \
  "v_mfma_f32_32x32x16_bf16 v[32:47], v[72:75], v[64:67], v[32:47]\n" \
  "v_mfma_f32_32x32x16_bf16 v[48:63], v[64:67], v[76:79], v[48:63]\n" \
  "ds_read_b128 v[204:207], %[ct]\n" \
  "ds_read_b128 v[112:115], v195\n" \
  "ds_read_b64_tr_b16 v[116:117], v196 offset:3072\n" \
  "ds_read_b64_tr_b16 v[118:119], v196 offset:3328\n" \
  "ds_read_b64_tr_b16 v[120:121], v197 offset:3072\n" \
  "ds_read_b64_tr_b16 v[122:123], v197 offset:3328\n" \
  "ds_read_b64_tr_b16 v[124:125], v198 offset:3072\n" \
  "ds_read_b64_tr_b16 v[126:127], v198 offset:3328\n" \
  "s_waitcnt lgkmcnt(14)\n" \
  "v_mfma_f32_32x32x16_bf16 v[16:31], v[84:87], v[80:83], v[16:31]\n" \
  "v_mfma_f32_32x32x16_bf16 v[32:47], v[88:91], v[80:83], v[32:47]\n" \
  "v_mfma_f32_32x32x16_bf16 v[48:63], v[80:83], v[92:95], v[48:63]\n" \
  "v_add_u32 v192, 86016, %[x0]\n" \
  "v_add_u32 v193, 86016, %[x1]\n" \
  "v_add_u32 v194, 86016, %[x2]\n" \
  "v_add_u32 v195, 86016, %[x3]\n" \
  "v_add_u32 v196, 86016, %[w0]\n" \
  "v_add_u32 v197, 86016, %[w1]\n" \
  "v_add_u32 v198, 86016, %[w2]\n" \
  "s_waitcnt lgkmcnt(7)\n" \
  "v_min_u32 v204, v204, v205\n" \
  "v_min3_u32 v204, v204, v206, v207\n" \
  "s_nop 0\n" \
  "v_readfirstlane_b32 s56, v204\n" \
  "s_cmp_ge_u32 s56, 4\n" \
  "s_cbranch_scc1 G3_%=\n" \
  "L3_%=:\n" \
  "s_sleep 2\n" \
  "ds_read_b128 v[204:207], %[ct]\n" \
  "s_waitcnt lgkmcnt(0)\n" \
  "v_min_u32 v204, v204, v205\n" \
  "v_min3_u32 v204, v204, v206, v207\n" \
  "s_nop 0\n" \
  "v_readfirstlane_b32 s56, v204\n" \
  "s_add_u32 s57, s57, 1\n" \
  "s_bitcmp1_b32 s57, 18\n" \
  "s_cbranch_scc1 G3_%=\n" \
  "s_cmp_lt_u32 s56, 4\n" \
  "s_cbranch_scc1 L3_%=\n" \
  "G3_%=:\n" \
  "ds_read_b128 v[128:131], v192\n" \
  "ds_read_b64_tr_b16 v[132:133], v196 offset:0\n" \
  "ds_read_b64_tr_b16 v[134:135], v196 offset:256\n" \
  "ds_read_b64_tr_b16 v[136:137], v197 offset:0\n" \
  "ds_read_b64_tr_b16 v[138:139], v197 offset:256\n" \
  "ds_read_b64_tr_b16 v[140:141], v198 offset:0\n" \
  "ds_read_b64_tr_b16 v[142:143], v198 offset:256\n" \
  "s_waitcnt lgkmcnt(14)\n" \
  "v_mfma_f32_32x32x16_bf16 v[16:31], v[100:103], v[96:99], v[16:31]\n" \
  "v_mfma_f32_32x32x16_bf16 v[32:47], v[104:107], v[96:99], v[32:47]\n" \
  "v_mfma_f32_32x32x16_bf16 v[48:63], v[96:99], v[108:111], v[48:63]\n" \
  "ds_read_b128 v[144:147], v193\n" \
  "ds_read_b64_tr_b16 v[148:149], v196 offset:1024\n" \
  "ds_read_b64_tr_b16 v[150:151], v196 offset:1280\n" \
  "ds_read_b64_tr_b16 v[152:153], v197 offset:1024\n" \
  "ds_read_b64_tr_b16 v[154:155], v197 offset:1280\n" \
  "ds_read_b64_tr_b16 v[156:157], v198 offset:1024\n" \
  "ds_read_b64_tr_b16 v[158:159], v198 offset:1280\n" \
  "s_waitcnt lgkmcnt(14)\n" \
  "v_mfma_f32_32x32x16_bf16 v[16:31], v[116:119], v[112:115], v[16:31]\n" \
  "v_mfma_f32_32x32x16_bf16 v[32:47], v[120:123], v[112:115], v[32:47]\n" \
  "v_mfma_f32_32x32x16_bf16 v[48:63], v[112:115], v[124:127], v[48:63]\n" \
  "v_mov_b32 v200, 3\n" \
  "ds_write_b32 %[fr], v200\n" \
  "ds_read_b128 v[160:163], v194\n" \
  "ds_read_b64_tr_b16 v[164:165], v196 offset:2048\n" \
  "ds_read_b64_tr_b16 v[166:167], v196 offset:2304\n" \
  "ds_read_b64_tr_b16 v[168:169], v197 offset:2048\n" \
  "ds_read_b64_tr_b16 v[170:171], v197 offset:2304\n" \
  "ds_read_b64_tr_b16 v[172:173], v198 offset:2048\n" \
  "ds_read_b64_tr_b16 v[174:175], v198 offset:2304\n" \
  "s_waitcnt lgkmcnt(14)\n" \
  "v_mfma_f32_32x32x16_bf16 v[16:31], v[132:135], v[128:131], v[16:31]\n" \
  "v_mfma_f32_32x32x16_bf16 v[32:47], v[136:139], v[128:131], v[32:47]\n" \
  "v_mfma_f32_32x32x16_bf16 v[48:63], v[128:131], v[140:143], v[48:63]\n" \
  "ds_read_b128 v[204:207], %[ct]\n" \
  "ds_read_b128 v[176:179], v195\n" \
  "ds_read_b64_tr_b16 v[180:181], v196 offset:3072\n" \
  "ds_read_b64_tr_b16 v[182:183], v196 offset:3328\n" \
  "ds_read_b64_tr_b16 v[184:185], v197 offset:3072\n" \
  "ds_read_b64_tr_b16 v[186:187], v197 offset:3328\n" \
  "ds_read_b64_tr_b16 v[188:189], v198 offset:3072\n" \
  "ds_read_b64_tr_b16 v[190:191], v198 offset:3328\n" \
  "s_waitcnt lgkmcnt(14)\n" \
  "v_mfma_f32_32x32x16_bf16 v[16:31], v[148:151], v[144:147], v[16:31]\n" \
  "v_mfma_f32_32x32x16_bf16 v[32:47], v[152:155], v[144:147], v[32:47]\n" \
  "v_mfma_f32_32x32x16_bf16 v[48:63], v[144:147], v[156:159], v[48:63]\n" \
  "v_add_u32 v192, 114688, %[x0]\n" \
  "v_add_u32 v193, 114688, %[x1]\n" \
  "v_add_u32 v194, 114688, %[x2]\n" \
  "v_add_u32 v195, 114688, %[x3]\n" \
  "v_add_u32 v196, 114688, %[w0]\n" \
  "v_add_u32 v197, 114688, %[w1]\n" \
  "v_add_u32 v198, 114688, %[w2]\n" \
  "s_waitcnt lgkmcnt(7)\n" \
  "v_min_u32 v204, v204, v205\n" \
  "v_min3_u32 v204, v204, v206, v207\n" \
  "s_nop 0\n" \
  "v_readfirstlane_b32 s56, v204\n" \
  "s_cmp_ge_u32 s56, 5\n" \
  "s_cbranch_scc1 G4_%=\n" \
  "L4_%=:\n" \
  "s_sleep 2\n" \
  "ds_read_b128 v[204:207], %[ct]\n" \
  "s_waitcnt lgkmcnt(0)\n" \
  "v_min_u32 v204, v204, v205\n" \
  "v_min3_u32 v204, v204, v206, v207\n" \
  "s_nop 0\n" \
  "v_readfirstlane_b32 s56, v204\n" \
  "s_add_u32 s57, s57, 1\n" \
  "s_bitcmp1_b32 s57, 18\n" \
  "s_cbranch_scc1 G4_%=\n" \
  "s_cmp_lt_u32 s56, 5\n" \
  "s_cbranch_scc1 L4_%=\n" \
  "G4_%=:\n" \
  "ds_read_b128 v[64:67], v192\n" \
  "ds_read_b64_tr_b16 v[68:69], v196 offset:0\n" \
  "ds_read_b64_tr_b16 v[70:71], v196 offset:256\n" \
  "ds_read_b64_tr_b16 v[72:73], v197 offset:0\n" \
  "ds_read_b64_tr_b16 v[74:75], v197 offset:256\n" \
  "ds_read_b64_tr_b16 v[76:77], v198 offset:0\n" \
  "ds_read_b64_tr_b16 v[78:79], v198 offset:256\n" \
  "s_waitcnt lgkmcnt(14)\n" \
  "v_mfma_f32_32x32x16_bf16 v[16:31], v[164:167], v[160:163], v[16:31]\n" \
  "v_mfma_f32_32x32x16_bf16 v[32:47], v[168:171], v[160:163], v[32:47]\n" \
  "v_mfma_f32_32x32x16_bf16 v[48:63], v[160:163], v[172:175], v[48:63]\n" \
  "ds_read_b128 v[80:83], v193\n" \
  "ds_read_b64_tr_b16 v[84:85], v196 offset:1024\n" \
  "ds_read_b64_tr_b16 v[86:87], v196 offset:1280\n" \
  "ds_read_b64_tr_b16 v[88:89], v197 offset:1024\n" \
  "ds_read_b64_tr_b16 v[90:91], v197 offset:1280\n" \
  "ds_read_b64_tr_b16 v[92:93], v198 offset:1024\n" \
  "ds_read_b64_tr_b16 v[94:95], v198 offset:1280\n" \
  "s_waitcnt lgkmcnt(14)\n" \
  "v_mfma_f32_32x32x16_bf16 v[16:31], v[180:183], v[176:179], v[16:31]\n" \
  "v_mfma_f32_32x32x16_bf16 v[32:47], v[184:187], v[176:179], v[32:47]\n" \
  "v_mfma_f32_32x32x16_bf16 v[48:63], v[176:179], v[188:191], v[48:63]\n" \
  "v_mov_b32 v200, 4\n" \
  "ds_write_b32 %[fr], v200\n" \
  "ds_read_b128 v[96:99], v194\n" \
  "ds_read_b64_tr_b16 v[100:101], v196 offset:2048\n" \
  "ds_read_b64_tr_b16 v[102:103], v196 offset:2304\n" \
  "ds_read_b64_tr_b16 v[104:105], v197 offset:2048\n" \
  "ds_read_b64_tr_b16 v[106:107], v197 offset:2304\n" \
  "ds_read_b64_tr_b16 v[108:109], v198 offset:2048\n" \
  "ds_read_b64_tr_b16 v[110:111], v198 offset:2304\n" \
  "s_waitcnt lgkmcnt(14)\n" \
  "v_mfma_f32_32x32x16_bf16 v[16:31], v[68:71], v[64:67], v[16:31]\n" \
  "v_mfma_f32_32x32x16_bf16 v[32:47], v[72:75], v[64:67], v[32:47]\n" \
  "v_mfma_f32_32x32x16_bf16 v[48:63], v[64:67], v[76:79], v[48:63]\n" \
  "ds_read_b128 v[204:207], %[ct]\n" \
  "ds_read_b128 v[112:115], v195\n" \
  "ds_read_b64_tr_b16 v[116:117], v196 offset:3072\n" \
  "ds_read_b64_tr_b16 v[118:119], v196 offset:3328\n" \
  "ds_read_b64_tr_b16 v[120:121], v197 offset:3072\n" \
  "ds_read_b64_tr_b16 v[122:123], v197 offset:3328\n" \
  "ds_read_b64_tr_b16 v[124:125], v198 offset:3072\n" \
  "ds_read_b64_tr_b16 v[126:127], v198 offset:3328\n" \
  "s_waitcnt lgkmcnt(14)\n" \
  "v_mfma_f32_32x32x16_bf16 v[16:31], v[84:87], v[80:83], v[16:31]\n" \
  "v_mfma_f32_32x32x16_bf16 v[32:47], v[88:91], v[80:83], v[32:47]\n" \
  "v_mfma_f32_32x32x16_bf16 v[48:63], v[80:83], v[92:95], v[48:63]\n" \
  "v_add_u32 v192, 0, %[x0]\n" \
  "v_add_u32 v193, 0, %[x1]\n" \
  "v_add_u32 v194, 0, %[x2]\n" \
  "v_add_u32 v195, 0, %[x3]\n" \
  "v_add_u32 v196, 0, %[w0]\n" \
  "v_add_u32 v197, 0, %[w1]\n" \
  "v_add_u32 v198, 0, %[w2]\n" \
  "s_waitcnt lgkmcnt(7)\n" \
  "v_min_u32 v204, v204, v205\n" \
  "v_min3_u32 v204, v204, v206, v207\n" \
  "s_nop 0\n" \
  "v_readfirstlane_b32 s56, v204\n" \
  "s_cmp_ge_u32 s56, 6\n" \
  "s_cbranch_scc1 G5_%=\n" \
  "L5_%=:\n" \
  "s_sleep 2\n" \
  "ds_read_b128 v[204:207], %[ct]\n" \
  "s_waitcnt lgkmcnt(0)\n" \
  "v_min_u32 v204, v204, v205\n" \
  "v_min3_u32 v204, v204, v206, v207\n" \
  "s_nop 0\n" \
  "v_readfirstlane_b32 s56, v204\n" \
  "s_add_u32 s57, s57, 1\n" \
  "s_bitcmp1_b32 s57, 18\n" \
  "s_cbranch_scc1 G5_%=\n" \
  "s_cmp_lt_u32 s56, 6\n" \
  "s_cbranch_scc1 L5_%=\n" \
  "G5_%=:\n" \
  "ds_read_b128 v[128:131], v192\n" \
  "ds_read_b64_tr_b16 v[132:133], v196 offset:0\n" \
  "ds_read_b64_tr_b16 v[134:135], v196 offset:256\n" \
  "ds_read_b64_tr_b16 v[136:137], v197 offset:0\n" \
  "ds_read_b64_tr_b16 v[138:139], v197 offset:256\n" \
  "ds_read_b64_tr_b16 v[140:141], v198 offset:0\n" \
  "ds_read_b64_tr_b16 v[142:143], v198 offset:256\n" \
  "s_waitcnt lgkmcnt(14)\n" \
  "v_mfma_f32_32x32x16_bf16 v[16:31], v[100:103], v[96:99], v[16:31]\n" \
  "v_mfma_f32_32x32x16_bf16 v[32:47], v[104:107], v[96:99], v[32:47]\n" \
  "v_mfma_f32_32x32x16_bf16 v[48:63], v[96:99], v[108:111], v[48:63]\n" \
  "ds_read_b128 v[144:147], v193\n" \
  "ds_read_b64_tr_b16 v[148:149], v196 offset:1024\n" \
  "ds_read_b64_tr_b16 v[150:151], v196 offset:1280\n" \
  "ds_read_b64_tr_b16 v[152:153], v197 offset:1024\n" \
  "ds_read_b64_tr_b16 v[154:155], v197 offset:1280\n" \
  "ds_read_b64_tr_b16 v[156:157], v198 offset:1024\n" \
  "ds_read_b64_tr_b16 v[158:159], v198 offset:1280\n" \
  "s_waitcnt lgkmcnt(14)\n" \
  "v_mfma_f32_32x32x16_bf16 v[16:31], v[116:119], v[112:115], v[16:31]\n" \
  "v_mfma_f32_32x32x16_bf16 v[32:47], v[120:123], v[112:115], v[32:47]\n" \
  "v_mfma_f32_32x32x16_bf16 v[48:63], v[112:115], v[124:127], v[48:63]\n" \
  "v_mov_b32 v200, 5\n" \
  "ds_write_b32 %[fr], v200\n" \
  "ds_read_b128 v[160:163], v194\n" \
  "ds_read_b64_tr_b16 v[164:165], v196 offset:2048\n" \
  "ds_read_b64_tr_b16 v[166:167], v196 offset:2304\n" \
  "ds_read_b64_tr_b16 v[168:169], v197 offset:2048\n" \
  "ds_read_b64_tr_b16 v[170:171], v197 offset:2304\n" \
  "ds_read_b64_tr_b16 v[172:173], v198 offset:2048\n" \
  "ds_read_b64_tr_b16 v[174:175], v198 offset:2304\n" \
  "s_waitcnt lgkmcnt(14)\n" \
  "v_mfma_f32_32x32x16_bf16 v[16:31], v[132:135], v[128:131], v[16:31]\n" \
  "v_mfma_f32_32x32x16_bf16 v[32:47], v[136:139], v[128:131], v[32:47]\n" \
  "v_mfma_f32_32x32x16_bf16 v[48:63], v[128:131], v[140:143], v[48:63]\n" \
  "ds_read_b128 v[204:207], %[ct]\n" \
  "ds_read_b128 v[176:179], v195\n" \
  "ds_read_b64_tr_b16 v[180:181], v196 offset:3072\n" \
  "ds_read_b64_tr_b16 v[182:183], v196 offset:3328\n" \
  "ds_read_b64_tr_b16 v[184:185], v197 offset:3072\n" \
  "ds_read_b64_tr_b16 v[186:187], v197 offset:3328\n" \
  "ds_read_b64_tr_b16 v[188:189], v198 offset:3072\n" \
  "ds_read_b64_tr_b16 v[190:191], v198 offset:3328\n" \
  "s_waitcnt lgkmcnt(14)\n" \
  "v_mfma_f32_32x32x16_bf16 v[16:31], v[148:151], v[144:147], v[16:31]\n" \
  "v_mfma_f32_32x32x16_bf16 v[32:47], v[152:155], v[144:147], v[32:47]\n" \
  "v_mfma_f32_32x32x16_bf16 v[48:63], v[144:147], v[156:159], v[48:63]\n" \
  "v_add_u32 v192, 28672, %[x0]\n" \
  "v_add_u32 v193, 28672, %[x1]\n" \
  "v_add_u32 v194, 28672, %[x2]\n" \
  "v_add_u32 v195, 28672, %[x3]\n" \
  "v_add_u32 v196, 28672, %[w0]\n" \
  "v_add_u32 v197, 28672, %[w1]\n" \
  "v_add_u32 v198, 28672, %[w2]\n" \
  "s_waitcnt lgkmcnt(7)\n" \
  "v_min_u32 v204, v204, v205\n" \
  "v_min3_u32 v204, v204, v206, v207\n" \
  "s_nop 0\n" \
  "v_readfirstlane_b32 s56, v204\n" \
  "s_cmp_ge_u32 s56, 7\n" \
  "s_cbranch_scc1 G6_%=\n" \
  "L6_%=:\n" \
  "s_sleep 2\n" \
  "ds_read_b128 v[204:207], %[ct]\n" \
  "s_waitcnt lgkmcnt(0)\n" \
  "v_min_u32 v204, v204, v205\n" \
  "v_min3_u32 v204, v204, v206, v207\n" \
  "s_nop 0\n" \
  "v_readfirstlane_b32 s56, v204\n" \
  "s_add_u32 s57, s57, 1\n" \
  "s_bitcmp1_b32 s57, 18\n" \
  "s_cbranch_scc1 G6_%=\n" \
  "s_cmp_lt_u32 s56, 7\n" \
  "s_cbranch_scc1 L6_%=\n" \
  "G6_%=:\n" \
  "ds_read_b128 v[64:67], v192\n" \
  "ds_read_b64_tr_b16 v[68:69], v196 offset:0\n" \
  "ds_read_b64_tr_b16 v[70:71], v196 offset:256\n" \
  "ds_read_b64_tr_b16 v[72:73], v197 offset:0\n" \
  "ds_read_b64_tr_b16 v[74:75], v197 offset:256\n" \
  "ds_read_b64_tr_b16 v[76:77], v198 offset:0\n" \
  "ds_read_b64_tr_b16 v[78:79], v198 offset:256\n" \
  "s_waitcnt lgkmcnt(14)\n" \
  "v_mfma_f32_32x32x16_bf16 v[16:31], v[164:167], v[160:163], v[16:31]\n" \
  "v_mfma_f32_32x32x16_bf16 v[32:47], v[168:171], v[160:163], v[32:47]\n" \
  "v_mfma_f32_32x32x16_bf16 v[48:63], v[160:163], v[172:175], v[48:63]\n" \
  "ds_read_b128 v[80:83], v193\n" \
  "ds_read_b64_tr_b16 v[84:85], v196 offset:1024\n" \
  "ds_read_b64_tr_b16 v[86:87], v196 offset:1280\n" \
  "ds_read_b64_tr_b16 v[88:89], v197 offset:1024\n" \
  "ds_read_b64_tr_b16 v[90:91], v197 offset:1280\n" \
  "ds_read_b64_tr_b16 v[92:93], v198 offset:1024\n" \
  "ds_read_b64_tr_b16 v[94:95], v198 offset:1280\n" \
  "s_waitcnt lgkmcnt(14)\n" \
  "v_mfma_f32_32x32x16_bf16 v[16:31], v[180:183], v[176:179], v[16:31]\n" \
  "v_mfma_f32_32x32x16_bf16 v[32:47], v[184:187], v[176:179], v[32:47]\n" \
  "v_mfma_f32_32x32x16_bf16 v[48:63], v[176:179], v[188:191], v[48:63]\n" \
  "ds_read_b128 v[96:99], v194\n" \
  "ds_read_b64_tr_b16 v[100:101], v196 offset:2048\n" \
  "ds_read_b64_tr_b16 v[102:103], v196 offset:2304\n" \
  "ds_read_b64_tr_b16 v[104:105], v197 offset:2048\n" \
  "ds_read_b64_tr_b16 v[106:107], v197 offset:2304\n" \
  "ds_read_b64_tr_b16 v[108:109], v198 offset:2048\n" \
  "ds_read_b64_tr_b16 v[110:111], v198 offset:2304\n" \
  "s_waitcnt lgkmcnt(14)\n" \
  "v_mfma_f32_32x32x16_bf16 v[16:31], v[68:71], v[64:67], v[16:31]\n" \
  "v_mfma_f32_32x32x16_bf16 v[32:47], v[72:75], v[64:67], v[32:47]\n" \
  "v_mfma_f32_32x32x16_bf16 v[48:63], v[64:67], v[76:79], v[48:63]\n" \
  "ds_read_b128 v[112:115], v195\n" \
  "ds_read_b64_tr_b16 v[116:117], v196 offset:3072\n" \
  "ds_read_b64_tr_b16 v[118:119], v196 offset:3328\n" \
  "ds_read_b64_tr_b16 v[120:121], v197 offset:3072\n" \
  "ds_read_b64_tr_b16 v[122:123], v197 offset:3328\n" \
  "ds_read_b64_tr_b16 v[124:125], v198 offset:3072\n" \
  "ds_read_b64_tr_b16 v[126:127], v198 offset:3328\n" \
  "s_waitcnt lgkmcnt(14)\n" \
  "v_mfma_f32_32x32x16_bf16 v[16:31], v[84:87], v[80:83], v[16:31]\n" \
  "v_mfma_f32_32x32x16_bf16 v[32:47], v[88:91], v[80:83], v[32:47]\n" \
  "v_mfma_f32_32x32x16_bf16 v[48:63], v[80:83], v[92:95], v[48:63]\n" \
  "s_waitcnt lgkmcnt(7)\n" \
  "v_mfma_f32_32x32x16_bf16 v[16:31], v[100:103], v[96:99], v[16:31]\n" \
  "v_mfma_f32_32x32x16_bf16 v[32:47], v[104:107], v[96:99], v[32:47]\n" \
  "v_mfma_f32_32x32x16_bf16 v[48:63], v[96:99], v[108:111], v[48:63]\n" \
  "s_waitcnt lgkmcnt(0)\n" \
  "v_mfma_f32_32x32x16_bf16 v[16:31], v[116:119], v[112:115], v[16:31]\n" \
  "v_mfma_f32_32x32x16_bf16 v[32:47], v[120:123], v[112:115], v[32:47]\n" \
  "v_mfma_f32_32x32x16_bf16 v[48:63], v[112:115], v[124:127], v[48:63]\n" \
  "s_nop 15\n"

#define BU_ASM \
  "s_mov_b32 s72, m0\n" \
  "s_mov_b32 s64, %[k0]\n" \
  "s_mov_b32 s74, 0\n" \
  "s_lshl_b32 s65, s64, 7\n" \
  "s_mov_b64 s[66:67], %[xa]\n" \
  "s_add_u32 s66, s66, s65\n" \
  "s_addc_u32 s67, s67, 0\n" \
  "s_mov_b64 s[68:69], %[bre]\n" \
  "s_add_u32 s68, s68, s65\n" \
  "s_addc_u32 s69, s69, 0\n" \
  "s_mov_b64 s[70:71], %[bim]\n" \
  "s_add_u32 s70, s70, s65\n" \
  "s_addc_u32 s71, s71, 0\n" \
  "s_add_u32 s64, s64, 1\n" \
  "s_cmp_eq_u32 s64, 7\n" \
  "s_cselect_b32 s64, 0, s64\n" \
  "s_add_u32 m0, %[lx], 0\n" \
  "s_nop 0\n" \
  "global_load_lds_dwordx4 %[xo0], s[66:67]\n" \
  "s_add_u32 m0, %[lx], 1024\n" \
  "s_nop 0\n" \
  "global_load_lds_dwordx4 %[xo1], s[66:67]\n" \
  "s_add_u32 m0, %[lx], 2048\n" \
  "s_nop 0\n" \
  "global_load_lds_dwordx4 %[xo2], s[66:67]\n" \
  "s_add_u32 m0, %[lx], 3072\n" \
  "s_nop 0\n" \
  "global_load_lds_dwordx4 %[xo3], s[66:67]\n" \
  "s_add_u32 m0, %[lb], 0\n" \
  "s_nop 0\n" \
  "global_load_lds_dwordx4 %[bo], s[68:69]\n" \
  "s_add_u32 m0, %[lb], 4096\n" \
  "s_nop 0\n" \
  "global_load_lds_dwordx4 %[bo], s[70:71]\n" \
  "s_lshl_b32 s65, s64, 7\n" \
  "s_mov_b64 s[66:67], %[xa]\n" \
  "s_add_u32 s66, s66, s65\n" \
  "s_addc_u32 s67, s67, 0\n" \
  "s_mov_b64 s[68:69], %[bre]\n" \
  "s_add_u32 s68, s68, s65\n" \
  "s_addc_u32 s69, s69, 0\n" \
  "s_mov_b64 s[70:71], %[bim]\n" \
  "s_add_u32 s70, s70, s65\n" \
  "s_addc_u32 s71, s71, 0\n" \
  "s_add_u32 s64, s64, 1\n" \
  "s_cmp_eq_u32 s64, 7\n" \
  "s_cselect_b32 s64, 0, s64\n" \
  "s_add_u32 m0, %[lx], 24576\n" \
  "s_nop 0\n" \
  "global_load_lds_dwordx4 %[xo0], s[66:67]\n" \
  "s_add_u32 m0, %[lx], 25600\n" \
  "s_nop 0\n" \
  "global_load_lds_dwordx4 %[xo1], s[66:67]\n" \
  "s_add_u32 m0, %[lx], 26624\n" \
  "s_nop 0\n" \
  "global_load_lds_dwordx4 %[xo2], s[66:67]\n" \
  "s_add_u32 m0, %[lx], 27648\n" \
  "s_nop 0\n" \
  "global_load_lds_dwordx4 %[xo3], s[66:67]\n" \
  "s_add_u32 m0, %[lb], 24576\n" \
  "s_nop 0\n" \
  "global_load_lds_dwordx4 %[bo], s[68:69]\n" \
  "s_add_u32 m0, %[lb], 28672\n" \
  "s_nop 0\n" \
  "global_load_lds_dwordx4 %[bo], s[70:71]\n" \
  "s_lshl_b32 s65, s64, 7\n" \
  "s_mov_b64 s[66:67], %[xa]\n" \
  "s_add_u32 s66, s66, s65\n" \
  "s_addc_u32 s67, s67, 0\n" \
  "s_mov_b64 s[68:69], %[bre]\n" \
  "s_add_u32 s68, s68, s65\n" \
  "s_addc_u32 s69, s69, 0\n" \
  "s_mov_b64 s[70:71], %[bim]\n" \
  "s_add_u32 s70, s70, s65\n" \
  "s_addc_u32 s71, s71, 0\n" \
  "s_add_u32 s64, s64, 1\n" \
  "s_cmp_eq_u32 s64, 7\n" \
  "s_cselect_b32 s64, 0, s64\n" \
  "s_add_u32 m0, %[lx], 49152\n" \
  "s_nop 0\n" \
  "global_load_lds_dwordx4 %[xo0], s[66:67]\n" \
  "s_add_u32 m0, %[lx], 50176\n" \
  "s_nop 0\n" \
  "global_load_lds_dwordx4 %[xo1], s[66:67]\n" \
  "s_add_u32 m0, %[lx], 51200\n" \
  "s_nop 0\n" \
  "global_load_lds_dwordx4 %[xo2], s[66:67]\n" \
  "s_add_u32 m0, %[lx], 52224\n" \
  "s_nop 0\n" \
  "global_load_lds_dwordx4 %[xo3], s[66:67]\n" \
  "s_add_u32 m0, %[lb], 49152\n" \
  "s_nop 0\n" \
  "global_load_lds_dwordx4 %[bo], s[68:69]\n" \
  "s_add_u32 m0, %[lb], 53248\n" \
  "s_nop 0\n" \
  "global_load_lds_dwordx4 %[bo], s[70:71]\n" \
  "v_add_u32 v96, 0, %[x0]\n" \
  "v_add_u32 v100, 0, %[b0]\n" \
  "v_add_u32 v97, 0, %[x1]\n" \
  "v_add_u32 v101, 0, %[b1]\n" \
  "v_add_u32 v98, 0, %[x2]\n" \
  "v_add_u32 v102, 0, %[b2]\n" \
  "v_add_u32 v99, 0, %[x3]\n" \
  "v_add_u32 v103, 0, %[b3]\n" \
  "s_waitcnt vmcnt(12)\n" \
  "v_mov_b32 v104, 1\n" \
  "ds_write_b32 %[me], v104\n" \
  "L0_%=:\n" \
  "ds_read_b128 v[106:109], %[ct] offset:0\n" \
  "s_waitcnt lgkmcnt(0)\n" \
  "v_min_u32 v106, v106, v107\n" \
  "v_min3_u32 v106, v106, v108, v109\n" \
  "s_nop 0\n" \
  "v_readfirstlane_b32 s73, v106\n" \
  "s_add_u32 s74, s74, 1\n" \
  "s_bitcmp1_b32 s74, 17\n" \
  "s_cbranch_scc1 L0g_%=\n" \
  "s_cmp_ge_u32 s73, 1\n" \
  "s_cbranch_scc1 L0g_%=\n" \
  "s_sleep 1\n" \
  "s_branch L0_%=\n" \
  "L0g_%=:\n" \
  "s_lshl_b32 s65, s64, 7\n" \
  "s_mov_b64 s[66:67], %[xa]\n" \
  "s_add_u32 s66, s66, s65\n" \
  "s_addc_u32 s67, s67, 0\n" \
  "s_mov_b64 s[68:69], %[bre]\n" \
  "s_add_u32 s68, s68, s65\n" \
  "s_addc_u32 s69, s69, 0\n" \
  "s_mov_b64 s[70:71], %[bim]\n" \
  "s_add_u32 s70, s70, s65\n" \
  "s_addc_u32 s71, s71, 0\n" \
  "s_add_u32 s64, s64, 1\n" \
  "s_cmp_eq_u32 s64, 7\n" \
  "s_cselect_b32 s64, 0, s64\n" \
  "ds_read_b128 v[48:51], v96\n" \
  "ds_read_b128 v[52:55], v100\n" \
  "ds_read_b128 v[56:59], v100 offset:4096\n" \
  "s_add_u32 m0, %[lx], 73728\n" \
  "s_nop 0\n" \
  "global_load_lds_dwordx4 %[xo0], s[66:67]\n" \
  "ds_read_b128 v[60:63], v97\n" \
  "ds_read_b128 v[64:67], v101\n" \
  "ds_read_b128 v[68:71], v101 offset:4096\n" \
  "s_add_u32 m0, %[lx], 74752\n" \
  "s_nop 0\n" \
  "global_load_lds_dwordx4 %[xo1], s[66:67]\n" \
  "s_waitcnt vmcnt(8)\n" \
  "v_mov_b32 v104, 2\n" \
  "ds_write_b32 %[me], v104\n" \
  "ds_read_b128 v[72:75], v98\n" \
  "ds_read_b128 v[76:79], v102\n" \
  "ds_read_b128 v[80:83], v102 offset:4096\n" \
  "s_waitcnt lgkmcnt(6)\n" \
  "v_mfma_f32_32x32x16_bf16 v[16:31], v[48:51], v[52:55], 0\n" \
  "v_mfma_f32_32x32x16_bf16 v[32:47], v[48:51], v[56:59], 0\n" \
  "s_add_u32 m0, %[lx], 75776\n" \
  "s_nop 0\n" \
  "global_load_lds_dwordx4 %[xo2], s[66:67]\n" \
  "s_add_u32 m0, %[lx], 76800\n" \
  "s_nop 0\n" \
  "global_load_lds_dwordx4 %[xo3], s[66:67]\n" \
  "ds_read_b128 v[106:109], %[ct]\n" \
  "ds_read_b128 v[84:87], v99\n" \
  "ds_read_b128 v[88:91], v103\n" \
  "ds_read_b128 v[92:95], v103 offset:4096\n" \
  "s_waitcnt lgkmcnt(6)\n" \
  "v_mfma_f32_32x32x16_bf16 v[16:31], v[60:63], v[64:67], v[16:31]\n" \
  "v_mfma_f32_32x32x16_bf16 v[32:47], v[60:63], v[68:71], v[32:47]\n" \
  "s_add_u32 m0, %[lb], 73728\n" \
  "s_nop 0\n" \
  "global_load_lds_dwordx4 %[bo], s[68:69]\n" \
  "s_add_u32 m0, %[lb], 77824\n" \
  "s_nop 0\n" \
  "global_load_lds_dwordx4 %[bo], s[70:71]\n" \
  "v_add_u32 v96, 24576, %[x0]\n" \
  "v_add_u32 v100, 24576, %[b0]\n" \
  "v_add_u32 v97, 24576, %[x1]\n" \
  "v_add_u32 v101, 24576, %[b1]\n" \
  "v_add_u32 v98, 24576, %[x2]\n" \
  "v_add_u32 v102, 24576, %[b2]\n" \
  "v_add_u32 v99, 24576, %[x3]\n" \
  "v_add_u32 v103, 24576, %[b3]\n" \
  "s_waitcnt lgkmcnt(3)\n" \
  "v_min_u32 v106, v106, v107\n" \
  "v_min3_u32 v106, v106, v108, v109\n" \
  "s_nop 0\n" \
  "v_readfirstlane_b32 s73, v106\n" \
  "s_cmp_ge_u32 s73, 2\n" \
  "s_cbranch_scc1 L1g_%=\n" \
  "L1_%=:\n" \
  "ds_read_b128 v[106:109], %[ct] offset:0\n" \
  "s_waitcnt lgkmcnt(0)\n" \
  "v_min_u32 v106, v106, v107\n" \
  "v_min3_u32 v106, v106, v108, v109\n" \
  "s_nop 0\n" \
  "v_readfirstlane_b32 s73, v106\n" \
  "s_add_u32 s74, s74, 1\n" \
  "s_bitcmp1_b32 s74, 17\n" \
  "s_cbranch_scc1 L1g_%=\n" \
  "s_cmp_ge_u32 s73, 2\n" \
  "s_cbranch_scc1 L1g_%=\n" \
  "s_sleep 1\n" \
  "s_branch L1_%=\n" \
  "L1g_%=:\n" \
  "s_lshl_b32 s65, s64, 7\n" \
  "s_mov_b64 s[66:67], %[xa]\n" \
  "s_add_u32 s66, s66, s65\n" \
  "s_addc_u32 s67, s67, 0\n" \
  "s_mov_b64 s[68:69], %[bre]\n" \
  "s_add_u32 s68, s68, s65\n" \
  "s_addc_u32 s69, s69, 0\n" \
  "s_mov_b64 s[70:71], %[bim]\n" \
  "s_add_u32 s70, s70, s65\n" \
  "s_addc_u32 s71, s71, 0\n" \
  "s_add_u32 s64, s64, 1\n" \
  "s_cmp_eq_u32 s64, 7\n" \
  "s_cselect_b32 s64, 0, s64\n" \
  "ds_read_b128 v[48:51], v96\n" \
  "ds_read_b128 v[52:55], v100\n" \
  "ds_read_b128 v[56:59], v100 offset:4096\n" \
  "s_waitcnt lgkmcnt(6)\n" \
  "v_mfma_f32_32x32x16_bf16 v[16:31], v[72:75], v[76:79], v[16:31]\n" \
  "v_mfma_f32_32x32x16_bf16 v[32:47], v[72:75], v[80:83], v[32:47]\n" \
  "ds_read_b128 v[60:63], v97\n" \
  "ds_read_b128 v[64:67], v101\n" \
  "ds_read_b128 v[68:71], v101 offset:4096\n" \
  "s_waitcnt lgkmcnt(6)\n" \
  "v_mfma_f32_32x32x16_bf16 v[16:31], v[84:87], v[88:91], v[16:31]\n" \
  "v_mfma_f32_32x32x16_bf16 v[32:47], v[84:87], v[92:95], v[32:47]\n" \
  "v_mov_b32 v105, 1\n" \
  "ds_write_b32 %[me], v105 offset:16\n" \
  "s_add_u32 m0, %[lx], 0\n" \
  "s_nop 0\n" \
  "global_load_lds_dwordx4 %[xo0], s[66:67]\n" \
  "s_add_u32 m0, %[lx], 1024\n" \
  "s_nop 0\n" \
  "global_load_lds_dwordx4 %[xo1], s[66:67]\n" \
  "s_waitcnt vmcnt(8)\n" \
  "v_mov_b32 v104, 3\n" \
  "ds_write_b32 %[me], v104\n" \
  "ds_read_b128 v[72:75], v98\n" \
  "ds_read_b128 v[76:79], v102\n" \
  "ds_read_b128 v[80:83], v102 offset:4096\n" \
  "s_waitcnt lgkmcnt(6)\n" \
  "v_mfma_f32_32x32x16_bf16 v[16:31], v[48:51], v[52:55], v[16:31]\n" \
  "v_mfma_f32_32x32x16_bf16 v[32:47], v[48:51], v[56:59], v[32:47]\n" \
  "s_add_u32 m0, %[lx], 2048\n" \
  "s_nop 0\n" \
  "global_load_lds_dwordx4 %[xo2], s[66:67]\n" \
  "s_add_u32 m0, %[lx], 3072\n" \
  "s_nop 0\n" \
  "global_load_lds_dwordx4 %[xo3], s[66:67]\n" \
  "ds_read_b128 v[106:109], %[ct]\n" \
  "ds_read_b128 v[84:87], v99\n" \
  "ds_read_b128 v[88:91], v103\n" \
  "ds_read_b128 v[92:95], v103 offset:4096\n" \
  "s_waitcnt lgkmcnt(6)\n" \
  "v_mfma_f32_32x32x16_bf16 v[16:31], v[60:63], v[64:67], v[16:31]\n" \
  "v_mfma_f32_32x32x16_bf16 v[32:47], v[60:63], v[68:71], v[32:47]\n" \
  "D1_%=:\n" \
  "ds_read_b128 v[110:113], %[ct] offset:16\n" \
  "s_waitcnt lgkmcnt(0)\n" \
  "v_min_u32 v110, v110, v111\n" \
  "v_min3_u32 v110, v110, v112, v113\n" \
  "s_nop 0\n" \
  "v_readfirstlane_b32 s73, v110\n" \
  "s_add_u32 s74, s74, 1\n" \
  "s_bitcmp1_b32 s74, 17\n" \
  "s_cbranch_scc1 D1g_%=\n" \
  "s_cmp_lt_u32 s73, 1\n" \
  "s_cbranch_scc1 D1_%=\n" \
  "D1g_%=:\n" \
  "s_add_u32 m0, %[lb], 0\n" \
  "s_nop 0\n" \
  "global_load_lds_dwordx4 %[bo], s[68:69]\n" \
  "s_add_u32 m0, %[lb], 4096\n" \
  "s_nop 0\n" \
  "global_load_lds_dwordx4 %[bo], s[70:71]\n" \
  "v_add_u32 v96, 49152, %[x0]\n" \
  "v_add_u32 v100, 49152, %[b0]\n" \
  "v_add_u32 v97, 49152, %[x1]\n" \
  "v_add_u32 v101, 49152, %[b1]\n" \
  "v_add_u32 v98, 49152, %[x2]\n" \
  "v_add_u32 v102, 49152, %[b2]\n" \
  "v_add_u32 v99, 49152, %[x3]\n" \
  "v_add_u32 v103, 49152, %[b3]\n" \
  "s_waitcnt lgkmcnt(3)\n" \
  "v_min_u32 v106, v106, v107\n" \
  "v_min3_u32 v106, v106, v108, v109\n" \
  "s_nop 0\n" \
  "v_readfirstlane_b32 s73, v106\n" \
  "s_cmp_ge_u32 s73, 3\n" \
  "s_cbranch_scc1 L2g_%=\n" \
  "L2_%=:\n" \
  "ds_read_b128 v[106:109], %[ct] offset:0\n" \
  "s_waitcnt lgkmcnt(0)\n" \
  "v_min_u32 v106, v106, v107\n" \
  "v_min3_u32 v106, v106, v108, v109\n" \
  "s_nop 0\n" \
  "v_readfirstlane_b32 s73, v106\n" \
  "s_add_u32 s74, s74, 1\n" \
  "s_bitcmp1_b32 s74, 17\n" \
  "s_cbranch_scc1 L2g_%=\n" \
  "s_cmp_ge_u32 s73, 3\n" \
  "s_cbranch_scc1 L2g_%=\n" \
  "s_sleep 1\n" \
  "s_branch L2_%=\n" \
  "L2g_%=:\n" \
  "s_lshl_b32 s65, s64, 7\n" \
  "s_mov_b64 s[66:67], %[xa]\n" \
  "s_add_u32 s66, s66, s65\n" \
  "s_addc_u32 s67, s67, 0\n" \
  "s_mov_b64 s[68:69], %[bre]\n" \
  "s_add_u32 s68, s68, s65\n" \
  "s_addc_u32 s69, s69, 0\n" \
  "s_mov_b64 s[70:71], %[bim]\n" \
  "s_add_u32 s70, s70, s65\n" \
  "s_addc_u32 s71, s71, 0\n" \
  "s_add_u32 s64, s64, 1\n" \
  "s_cmp_eq_u32 s64, 7\n" \
  "s_cselect_b32 s64, 0, s64\n" \
  "ds_read_b128 v[48:51], v96\n" \
  "ds_read_b128 v[52:55], v100\n" \
  "ds_read_b128 v[56:59], v100 offset:4096\n" \
  "s_waitcnt lgkmcnt(6)\n" \
  "v_mfma_f32_32x32x16_bf16 v[16:31], v[72:75], v[76:79], v[16:31]\n" \
  "v_mfma_f32_32x32x16_bf16 v[32:47], v[72:75], v[80:83], v[32:47]\n" \
  "ds_read_b128 v[60:63], v97\n" \
  "ds_read_b128 v[64:67], v101\n" \
  "ds_read_b128 v[68:71], v101 offset:4096\n" \
  "s_waitcnt lgkmcnt(6)\n" \
  "v_mfma_f32_32x32x16_bf16 v[16:31], v[84:87], v[88:91], v[16:31]\n" \
  "v_mfma_f32_32x32x16_bf16 v[32:47], v[84:87], v[92:95], v[32:47]\n" \
  "v_mov_b32 v105, 2\n" \
  "ds_write_b32 %[me], v105 offset:16\n" \
  "s_add_u32 m0, %[lx], 24576\n" \
  "s_nop 0\n" \
  "global_load_lds_dwordx4 %[xo0], s[66:67]\n" \
  "s_add_u32 m0, %[lx], 25600\n" \
  "s_nop 0\n" \
  "global_load_lds_dwordx4 %[xo1], s[66:67]\n" \
  "s_waitcnt vmcnt(8)\n" \
  "v_mov_b32 v104, 4\n" \
  "ds_write_b32 %[me], v104\n" \
  "ds_read_b128 v[72:75], v98\n" \
  "ds_read_b128 v[76:79], v102\n" \
  "ds_read_b128 v[80:83], v102 offset:4096\n" \
  "s_waitcnt lgkmcnt(6)\n" \
  "v_mfma_f32_32x32x16_bf16 v[16:31], v[48:51], v[52:55], v[16:31]\n" \
  "v_mfma_f32_32x32x16_bf16 v[32:47], v[48:51], v[56:59], v[32:47]\n" \
  "s_add_u32 m0, %[lx], 26624\n" \
  "s_nop 0\n" \
  "global_load_lds_dwordx4 %[xo2], s[66:67]\n" \
  "s_add_u32 m0, %[lx], 27648\n" \
  "s_nop 0\n" \
  "global_load_lds_dwordx4 %[xo3], s[66:67]\n" \
  "ds_read_b128 v[106:109], %[ct]\n" \
  "ds_read_b128 v[84:87], v99\n" \
  "ds_read_b128 v[88:91], v103\n" \
  "ds_read_b128 v[92:95], v103 offset:4096\n" \
  "s_waitcnt lgkmcnt(6)\n" \
  "v_mfma_f32_32x32x16_bf16 v[16:31], v[60:63], v[64:67], v[16:31]\n" \
  "v_mfma_f32_32x32x16_bf16 v[32:47], v[60:63], v[68:71], v[32:47]\n" \
  "D2_%=:\n" \
  "ds_read_b128 v[110:113], %[ct] offset:16\n" \
  "s_waitcnt lgkmcnt(0)\n" \
  "v_min_u32 v110, v110, v111\n" \
  "v_min3_u32 v110, v110, v112, v113\n" \
  "s_nop 0\n" \
  "v_readfirstlane_b32 s73, v110\n" \
  "s_add_u32 s74, s74, 1\n" \
  "s_bitcmp1_b32 s74, 17\n" \
  "s_cbranch_scc1 D2g_%=\n" \
  "s_cmp_lt_u32 s73, 2\n" \
  "s_cbranch_scc1 D2_%=\n" \
  "D2g_%=:\n" \
  "s_add_u32 m0, %[lb], 24576\n" \
  "s_nop 0\n" \
  "global_load_lds_dwordx4 %[bo], s[68:69]\n" \
  "s_add_u32 m0, %[lb], 28672\n" \
  "s_nop 0\n" \
  "global_load_lds_dwordx4 %[bo], s[70:71]\n" \
  "v_add_u32 v96, 73728, %[x0]\n" \
  "v_add_u32 v100, 73728, %[b0]\n" \
  "v_add_u32 v97, 73728, %[x1]\n" \
  "v_add_u32 v101, 73728, %[b1]\n" \
  "v_add_u32 v98, 73728, %[x2]\n" \
  "v_add_u32 v102, 73728, %[b2]\n" \
  "v_add_u32 v99, 73728, %[x3]\n" \
  "v_add_u32 v103, 73728, %[b3]\n" \
  "s_waitcnt lgkmcnt(3)\n" \
  "v_min_u32 v106, v106, v107\n" \
  "v_min3_u32 v106, v106, v108, v109\n" \
  "s_nop 0\n" \
  "v_readfirstlane_b32 s73, v106\n" \
  "s_cmp_ge_u32 s73, 4\n" \
  "s_cbranch_scc1 L3g_%=\n" \
  "L3_%=:\n" \
  "ds_read_b128 v[106:109], %[ct] offset:0\n" \
  "s_waitcnt lgkmcnt(0)\n" \
  "v_min_u32 v106, v106, v107\n" \
  "v_min3_u32 v106, v106, v108, v109\n" \
  "s_nop 0\n" \
  "v_readfirstlane_b32 s73, v106\n" \
  "s_add_u32 s74, s74, 1\n" \
  "s_bitcmp1_b32 s74, 17\n" \
  "s_cbranch_scc1 L3g_%=\n" \
  "s_cmp_ge_u32 s73, 4\n" \
  "s_cbranch_scc1 L3g_%=\n" \
  "s_sleep 1\n" \
  "s_branch L3_%=\n" \
  "L3g_%=:\n" \
  "s_lshl_b32 s65, s64, 7\n" \
  "s_mov_b64 s[66:67], %[xa]\n" \
  "s_add_u32 s66, s66, s65\n" \
  "s_addc_u32 s67, s67, 0\n" \
  "s_mov_b64 s[68:69], %[bre]\n" \
  "s_add_u32 s68, s68, s65\n" \
  "s_addc_u32 s69, s69, 0\n" \
  "s_mov_b64 s[70:71], %[bim]\n" \
  "s_add_u32 s70, s70, s65\n" \
  "s_addc_u32 s71, s71, 0\n" \
  "s_add_u32 s64, s64, 1\n" \
  "s_cmp_eq_u32 s64, 7\n" \
  "s_cselect_b32 s64, 0, s64\n" \
  "ds_read_b128 v[48:51], v96\n" \
  "ds_read_b128 v[52:55], v100\n" \
  "ds_read_b128 v[56:59], v100 offset:4096\n" \
  "s_waitcnt lgkmcnt(6)\n" \
  "v_mfma_f32_32x32x16_bf16 v[16:31], v[72:75], v[76:79], v[16:31]\n" \
  "v_mfma_f32_32x32x16_bf16 v[32:47], v[72:75], v[80:83], v[32:47]\n" \
  "ds_read_b128 v[60:63], v97\n" \
  "ds_read_b128 v[64:67], v101\n" \
  "ds_read_b128 v[68:71], v101 offset:4096\n" \
  "s_waitcnt lgkmcnt(6)\n" \
  "v_mfma_f32_32x32x16_bf16 v[16:31], v[84:87], v[88:91], v[16:31]\n" \
  "v_mfma_f32_32x32x16_bf16 v[32:47], v[84:87], v[92:95], v[32:47]\n" \
  "v_mov_b32 v105, 3\n" \
  "ds_write_b32 %[me], v105 offset:16\n" \
  "s_add_u32 m0, %[lx], 49152\n" \
  "s_nop 0\n" \
  "global_load_lds_dwordx4 %[xo0], s[66:67]\n" \
  "s_add_u32 m0, %[lx], 50176\n" \
  "s_nop 0\n" \
  "global_load_lds_dwordx4 %[xo1], s[66:67]\n" \
  "s_waitcnt vmcnt(8)\n" \
  "v_mov_b32 v104, 5\n" \
  "ds_write_b32 %[me], v104\n" \
  "ds_read_b128 v[72:75], v98\n" \
  "ds_read_b128 v[76:79], v102\n" \
  "ds_read_b128 v[80:83], v102 offset:4096\n" \
  "s_waitcnt lgkmcnt(6)\n" \
  "v_mfma_f32_32x32x16_bf16 v[16:31], v[48:51], v[52:55], v[16:31]\n" \
  "v_mfma_f32_32x32x16_bf16 v[32:47], v[48:51], v[56:59], v[32:47]\n" \
  "s_add_u32 m0, %[lx], 51200\n" \
  "s_nop 0\n" \
  "global_load_lds_dwordx4 %[xo2], s[66:67]\n" \
  "s_add_u32 m0, %[lx], 52224\n" \
  "s_nop 0\n" \
  "global_load_lds_dwordx4 %[xo3], s[66:67]\n" \
  "ds_read_b128 v[106:109], %[ct]\n" \
  "ds_read_b128 v[84:87], v99\n" \
  "ds_read_b128 v[88:91], v103\n" \
  "ds_read_b128 v[92:95], v103 offset:4096\n" \
  "s_waitcnt lgkmcnt(6)\n" \
  "v_mfma_f32_32x32x16_bf16 v[16:31], v[60:63], v[64:67], v[16:31]\n" \
  "v_mfma_f32_32x32x16_bf16 v[32:47], v[60:63], v[68:71], v[32:47]\n" \
  "D3_%=:\n" \
  "ds_read_b128 v[110:113], %[ct] offset:16\n" \
  "s_waitcnt lgkmcnt(0)\n" \
  "v_min_u32 v110, v110, v111\n" \
  "v_min3_u32 v110, v110, v112, v113\n" \
  "s_nop 0\n" \
  "v_readfirstlane_b32 s73, v110\n" \
  "s_add_u32 s74, s74, 1\n" \
  "s_bitcmp1_b32 s74, 17\n" \
  "s_cbranch_scc1 D3g_%=\n" \
  "s_cmp_lt_u32 s73, 3\n" \
  "s_cbranch_scc1 D3_%=\n" \
  "D3g_%=:\n" \
  "s_add_u32 m0, %[lb], 49152\n" \
  "s_nop 0\n" \
  "global_load_lds_dwordx4 %[bo], s[68:69]\n" \
  "s_add_u32 m0, %[lb], 53248\n" \
  "s_nop 0\n" \
  "global_load_lds_dwordx4 %[bo], s[70:71]\n" \
  "v_add_u32 v96, 0, %[x0]\n" \
  "v_add_u32 v100, 0, %[b0]\n" \
  "v_add_u32 v97, 0, %[x1]\n" \
  "v_add_u32 v101, 0, %[b1]\n" \
  "v_add_u32 v98, 0, %[x2]\n" \
  "v_add_u32 v102, 0, %[b2]\n" \
  "v_add_u32 v99, 0, %[x3]\n" \
  "v_add_u32 v103, 0, %[b3]\n" \
  "s_waitcnt lgkmcnt(3)\n" \
  "v_min_u32 v106, v106, v107\n" \
  "v_min3_u32 v106, v106, v108, v109\n" \
  "s_nop 0\n" \
  "v_readfirstlane_b32 s73, v106\n" \
  "s_cmp_ge_u32 s73, 5\n" \
  "s_cbranch_scc1 L4g_%=\n" \
  "L4_%=:\n" \
  "ds_read_b128 v[106:109], %[ct] offset:0\n" \
  "s_waitcnt lgkmcnt(0)\n" \
  "v_min_u32 v106, v106, v107\n" \
  "v_min3_u32 v106, v106, v108, v109\n" \
  "s_nop 0\n" \
  "v_readfirstlane_b32 s73, v106\n" \
  "s_add_u32 s74, s74, 1\n" \
  "s_bitcmp1_b32 s74, 17\n" \
  "s_cbranch_scc1 L4g_%=\n" \
  "s_cmp_ge_u32 s73, 5\n" \
  "s_cbranch_scc1 L4g_%=\n" \
  "s_sleep 1\n" \
  "s_branch L4_%=\n" \
  "L4g_%=:\n" \
  "ds_read_b128 v[48:51], v96\n" \
  "ds_read_b128 v[52:55], v100\n" \
  "ds_read_b128 v[56:59], v100 offset:4096\n" \
  "s_waitcnt lgkmcnt(6)\n" \
  "v_mfma_f32_32x32x16_bf16 v[16:31], v[72:75], v[76:79], v[16:31]\n" \
  "v_mfma_f32_32x32x16_bf16 v[32:47], v[72:75], v[80:83], v[32:47]\n" \
  "ds_read_b128 v[60:63], v97\n" \
  "ds_read_b128 v[64:67], v101\n" \
  "ds_read_b128 v[68:71], v101 offset:4096\n" \
  "s_waitcnt lgkmcnt(6)\n" \
  "v_mfma_f32_32x32x16_bf16 v[16:31], v[84:87], v[88:91], v[16:31]\n" \
  "v_mfma_f32_32x32x16_bf16 v[32:47], v[84:87], v[92:95], v[32:47]\n" \
  "v_mov_b32 v105, 4\n" \
  "ds_write_b32 %[me], v105 offset:16\n" \
  "s_waitcnt vmcnt(6)\n" \
  "v_mov_b32 v104, 6\n" \
  "ds_write_b32 %[me], v104\n" \
  "ds_read_b128 v[72:75], v98\n" \
  "ds_read_b128 v[76:79], v102\n" \
  "ds_read_b128 v[80:83], v102 offset:4096\n" \
  "s_waitcnt lgkmcnt(6)\n" \
  "v_mfma_f32_32x32x16_bf16 v[16:31], v[48:51], v[52:55], v[16:31]\n" \
  "v_mfma_f32_32x32x16_bf16 v[32:47], v[48:51], v[56:59], v[32:47]\n" \
  "ds_read_b128 v[106:109], %[ct]\n" \
  "ds_read_b128 v[84:87], v99\n" \
  "ds_read_b128 v[88:91], v103\n" \
  "ds_read_b128 v[92:95], v103 offset:4096\n" \
  "s_waitcnt lgkmcnt(6)\n" \
  "v_mfma_f32_32x32x16_bf16 v[16:31], v[60:63], v[64:67], v[16:31]\n" \
  "v_mfma_f32_32x32x16_bf16 v[32:47], v[60:63], v[68:71], v[32:47]\n" \
  "v_add_u32 v96, 24576, %[x0]\n" \
  "v_add_u32 v100, 24576, %[b0]\n" \
  "v_add_u32 v97, 24576, %[x1]\n" \
  "v_add_u32 v101, 24576, %[b1]\n" \
  "v_add_u32 v98, 24576, %[x2]\n" \
  "v_add_u32 v102, 24576, %[b2]\n" \
  "v_add_u32 v99, 24576, %[x3]\n" \
  "v_add_u32 v103, 24576, %[b3]\n" \
  "s_waitcnt lgkmcnt(3)\n" \
  "v_min_u32 v106, v106, v107\n" \
  "v_min3_u32 v106, v106, v108, v109\n" \
  "s_nop 0\n" \
  "v_readfirstlane_b32 s73, v106\n" \
  "s_cmp_ge_u32 s73, 6\n" \
  "s_cbranch_scc1 L5g_%=\n" \
  "L5_%=:\n" \
  "ds_read_b128 v[106:109], %[ct] offset:0\n" \
  "s_waitcnt lgkmcnt(0)\n" \
  "v_min_u32 v106, v106, v107\n" \
  "v_min3_u32 v106, v106, v108, v109\n" \
  "s_nop 0\n" \
  "v_readfirstlane_b32 s73, v106\n" \
  "s_add_u32 s74, s74, 1\n" \
  "s_bitcmp1_b32 s74, 17\n" \
  "s_cbranch_scc1 L5g_%=\n" \
  "s_cmp_ge_u32 s73, 6\n" \
  "s_cbranch_scc1 L5g_%=\n" \
  "s_sleep 1\n" \
  "s_branch L5_%=\n" \
  "L5g_%=:\n" \
  "ds_read_b128 v[48:51], v96\n" \
  "ds_read_b128 v[52:55], v100\n" \
  "ds_read_b128 v[56:59], v100 offset:4096\n" \
  "s_waitcnt lgkmcnt(6)\n" \
  "v_mfma_f32_32x32x16_bf16 v[16:31], v[72:75], v[76:79], v[16:31]\n" \
  "v_mfma_f32_32x32x16_bf16 v[32:47], v[72:75], v[80:83], v[32:47]\n" \
  "ds_read_b128 v[60:63], v97\n" \
  "ds_read_b128 v[64:67], v101\n" \
  "ds_read_b128 v[68:71], v101 offset:4096\n" \
  "s_waitcnt lgkmcnt(6)\n" \
  "v_mfma_f32_32x32x16_bf16 v[16:31], v[84:87], v[88:91], v[16:31]\n" \
  "v_mfma_f32_32x32x16_bf16 v[32:47], v[84:87], v[92:95], v[32:47]\n" \
  "v_mov_b32 v105, 5\n" \
  "ds_write_b32 %[me], v105 offset:16\n" \
  "s_waitcnt vmcnt(0)\n" \
  "v_mov_b32 v104, 7\n" \
  "ds_write_b32 %[me], v104\n" \
  "ds_read_b128 v[72:75], v98\n" \
  "ds_read_b128 v[76:79], v102\n" \
  "ds_read_b128 v[80:83], v102 offset:4096\n" \
  "s_waitcnt lgkmcnt(6)\n" \
  "v_mfma_f32_32x32x16_bf16 v[16:31], v[48:51], v[52:55], v[16:31]\n" \
  "v_mfma_f32_32x32x16_bf16 v[32:47], v[48:51], v[56:59], v[32:47]\n" \
  "ds_read_b128 v[106:109], %[ct]\n" \
  "ds_read_b128 v[84:87], v99\n" \
  "ds_read_b128 v[88:91], v103\n" \
  "ds_read_b128 v[92:95], v103 offset:4096\n" \
  "s_waitcnt lgkmcnt(6)\n" \
  "v_mfma_f32_32x32x16_bf16 v[16:31], v[60:63], v[64:67], v[16:31]\n" \
  "v_mfma_f32_32x32x16_bf16 v[32:47], v[60:63], v[68:71], v[32:47]\n" \
  "v_add_u32 v96, 49152, %[x0]\n" \
  "v_add_u32 v100, 49152, %[b0]\n" \
  "v_add_u32 v97, 49152, %[x1]\n" \
  "v_add_u32 v101, 49152, %[b1]\n" \
  "v_add_u32 v98, 49152, %[x2]\n" \
  "v_add_u32 v102, 49152, %[b2]\n" \
  "v_add_u32 v99, 49152, %[x3]\n" \
  "v_add_u32 v103, 49152, %[b3]\n" \
  "s_waitcnt lgkmcnt(3)\n" \
  "v_min_u32 v106, v106, v107\n" \
  "v_min3_u32 v106, v106, v108, v109\n" \
  "s_nop 0\n" \
  "v_readfirstlane_b32 s73, v106\n" \
  "s_cmp_ge_u32 s73, 7\n" \
  "s_cbranch_scc1 L6g_%=\n" \
  "L6_%=:\n" \
  "ds_read_b128 v[106:109], %[ct] offset:0\n" \
  "s_waitcnt lgkmcnt(0)\n" \
  "v_min_u32 v106, v106, v107\n" \
  "v_min3_u32 v106, v106, v108, v109\n" \
  "s_nop 0\n" \
  "v_readfirstlane_b32 s73, v106\n" \
  "s_add_u32 s74, s74, 1\n" \
  "s_bitcmp1_b32 s74, 17\n" \
  "s_cbranch_scc1 L6g_%=\n" \
  "s_cmp_ge_u32 s73, 7\n" \
  "s_cbranch_scc1 L6g_%=\n" \
  "s_sleep 1\n" \
  "s_branch L6_%=\n" \
  "L6g_%=:\n" \
  "ds_read_b128 v[48:51], v96\n" \
  "ds_read_b128 v[52:55], v100\n" \
  "ds_read_b128 v[56:59], v100 offset:4096\n" \
  "s_waitcnt lgkmcnt(6)\n" \
  "v_mfma_f32_32x32x16_bf16 v[16:31], v[72:75], v[76:79], v[16:31]\n" \
  "v_mfma_f32_32x32x16_bf16 v[32:47], v[72:75], v[80:83], v[32:47]\n" \
  "ds_read_b128 v[60:63], v97\n" \
  "ds_read_b128 v[64:67], v101\n" \
  "ds_read_b128 v[68:71], v101 offset:4096\n" \
  "s_waitcnt lgkmcnt(6)\n" \
  "v_mfma_f32_32x32x16_bf16 v[16:31], v[84:87], v[88:91], v[16:31]\n" \
  "v_mfma_f32_32x32x16_bf16 v[32:47], v[84:87], v[92:95], v[32:47]\n" \
  "v_mov_b32 v105, 6\n" \
  "ds_write_b32 %[me], v105 offset:16\n" \
  "ds_read_b128 v[72:75], v98\n" \
  "ds_read_b128 v[76:79], v102\n" \
  "ds_read_b128 v[80:83], v102 offset:4096\n" \
  "s_waitcnt lgkmcnt(6)\n" \
  "v_mfma_f32_32x32x16_bf16 v[16:31], v[48:51], v[52:55], v[16:31]\n" \
  "v_mfma_f32_32x32x16_bf16 v[32:47], v[48:51], v[56:59], v[32:47]\n" \
  "ds_read_b128 v[84:87], v99\n" \
  "ds_read_b128 v[88:91], v103\n" \
  "ds_read_b128 v[92:95], v103 offset:4096\n" \
  "s_waitcnt lgkmcnt(6)\n" \
  "v_mfma_f32_32x32x16_bf16 v[16:31], v[60:63], v[64:67], v[16:31]\n" \
  "v_mfma_f32_32x32x16_bf16 v[32:47], v[60:63], v[68:71], v[32:47]\n" \
  "s_waitcnt lgkmcnt(3)\n" \
  "v_mfma_f32_32x32x16_bf16 v[16:31], v[72:75], v[76:79], v[16:31]\n" \
  "v_mfma_f32_32x32x16_bf16 v[32:47], v[72:75], v[80:83], v[32:47]\n" \
  "s_waitcnt lgkmcnt(0)\n" \
  "v_mfma_f32_32x32x16_bf16 v[16:31], v[84:87], v[88:91], v[16:31]\n" \
  "v_mfma_f32_32x32x16_bf16 v[32:47], v[84:87], v[92:95], v[32:47]\n" \
  "s_mov_b32 m0, s72\n" \
  "s_nop 15\n"
#define BU_CLOBBERS "v48", "v49", "v50", "v51", "v52", "v53", "v54", "v55", "v56", "v57", "v58", "v59", "v60", "v61", "v62", "v63", "v64", "v65", "v66", "v67", "v68", "v69", "v70", "v71", "v72", "v73", "v74", "v75", "v76", "v77", "v78", "v79", "v80", "v81", "v82", "v83", "v84", "v85", "v86", "v87", "v88", "v89", "v90", "v91", "v92", "v93", "v94", "v95", "v96", "v97", "v98", "v99", "v100", "v101", "v102", "v103", "v104", "v105", "v106", "v107", "v108", "v109", "v110", "v111", "v112", "v113", "s64", "s65", "s66", "s67", "s68", "s69", "s70", "s71", "s72", "s73", "s74", "s75", "scc", "memory"
#define Y_ASM \
  "s_mov_b32 s68, 0\n" \
  "s_mov_b32 s66, 0\n" \
  "v_xor_b32 v8, 0, %[c0]\n" \
  "v_xor_b32 v9, 32, %[c0]\n" \
  "v_xor_b32 v10, 64, %[c0]\n" \
  "v_xor_b32 v11, 96, %[c0]\n" \
  "W0_%=:\n" \
  "s_bitcmp1_b32 s68, 0\n" \
  "s_cbranch_scc1 R0_%=\n" \
  "global_load_dword v12, %[fa], %[fb] sc1\n" \
  "s_waitcnt vmcnt(0)\n" \
  "v_cmp_eq_u32 vcc, %[tg], v12\n" \
  "s_nop 1\n" \
  "s_mov_b32 s68, vcc_lo\n" \
  "s_add_u32 s66, s66, 1\n" \
  "s_bitcmp1_b32 s66, 17\n" \
  "s_cbranch_scc1 R0_%=\n" \
  "s_bitcmp1_b32 s68, 0\n" \
  "s_cbranch_scc1 R0_%=\n" \
  "s_sleep 2\n" \
  "s_branch W0_%=\n" \
  "R0_%=:\n" \
  "s_mov_b64 s[64:65], %[hb]\n" \
  "s_add_u32 s64, s64, 0\n" \
  "s_addc_u32 s65, s65, 0\n" \
  "global_load_dwordx4 v[32:35], %[lo], s[64:65] sc1\n" \
  "global_load_dwordx4 v[36:39], %[lo], s[64:65] offset:1024 sc1\n" \
  "s_sleep 2\n" \
  "s_add_u32 s64, s64, 28672\n" \
  "s_addc_u32 s65, s65, 0\n" \
  "global_load_dwordx4 v[40:43], %[lo], s[64:65] sc1\n" \
  "global_load_dwordx4 v[44:47], %[lo], s[64:65] offset:1024 sc1\n" \
  "s_sleep 2\n" \
  "W1_%=:\n" \
  "s_bitcmp1_b32 s68, 1\n" \
  "s_cbranch_scc1 R1_%=\n" \
  "global_load_dword v12, %[fa], %[fb] sc1\n" \
  "s_waitcnt vmcnt(0)\n" \
  "v_cmp_eq_u32 vcc, %[tg], v12\n" \
  "s_nop 1\n" \
  "s_mov_b32 s68, vcc_lo\n" \
  "s_add_u32 s66, s66, 1\n" \
  "s_bitcmp1_b32 s66, 17\n" \
  "s_cbranch_scc1 R1_%=\n" \
  "s_bitcmp1_b32 s68, 1\n" \
  "s_cbranch_scc1 R1_%=\n" \
  "s_sleep 2\n" \
  "s_branch W1_%=\n" \
  "R1_%=:\n" \
  "s_mov_b64 s[64:65], %[hb]\n" \
  "s_add_u32 s64, s64, 2048\n" \
  "s_addc_u32 s65, s65, 0\n" \
  "global_load_dwordx4 v[48:51], %[lo], s[64:65] sc1\n" \
  "global_load_dwordx4 v[52:55], %[lo], s[64:65] offset:1024 sc1\n" \
  "s_sleep 2\n" \
  "s_add_u32 s64, s64, 28672\n" \
  "s_addc_u32 s65, s65, 0\n" \
  "global_load_dwordx4 v[56:59], %[lo], s[64:65] sc1\n" \
  "global_load_dwordx4 v[60:63], %[lo], s[64:65] offset:1024 sc1\n" \
  "s_sleep 2\n" \
  "W2_%=:\n" \
  "s_bitcmp1_b32 s68, 2\n" \
  "s_cbranch_scc1 R2_%=\n" \
  "global_load_dword v12, %[fa], %[fb] sc1\n" \
  "s_waitcnt vmcnt(0)\n" \
  "v_cmp_eq_u32 vcc, %[tg], v12\n" \
  "s_nop 1\n" \
  "s_mov_b32 s68, vcc_lo\n" \
  "s_add_u32 s66, s66, 1\n" \
  "s_bitcmp1_b32 s66, 17\n" \
  "s_cbranch_scc1 R2_%=\n" \
  "s_bitcmp1_b32 s68, 2\n" \
  "s_cbranch_scc1 R2_%=\n" \
  "s_sleep 2\n" \
  "s_branch W2_%=\n" \
  "R2_%=:\n" \
  "s_mov_b64 s[64:65], %[hb]\n" \
  "s_add_u32 s64, s64, 4096\n" \
  "s_addc_u32 s65, s65, 0\n" \
  "global_load_dwordx4 v[64:67], %[lo], s[64:65] sc1\n" \
  "global_load_dwordx4 v[68:71], %[lo], s[64:65] offset:1024 sc1\n" \
  "s_sleep 2\n" \
  "s_add_u32 s64, s64, 28672\n" \
  "s_addc_u32 s65, s65, 0\n" \
  "global_load_dwordx4 v[72:75], %[lo], s[64:65] sc1\n" \
  "global_load_dwordx4 v[76:79], %[lo], s[64:65] offset:1024 sc1\n" \
  "s_sleep 2\n" \
  "W3_%=:\n" \
  "s_bitcmp1_b32 s68, 3\n" \
  "s_cbranch_scc1 R3_%=\n" \
  "global_load_dword v12, %[fa], %[fb] sc1\n" \
  "s_waitcnt vmcnt(0)\n" \
  "v_cmp_eq_u32 vcc, %[tg], v12\n" \
  "s_nop 1\n" \
  "s_mov_b32 s68, vcc_lo\n" \
  "s_add_u32 s66, s66, 1\n" \
  "s_bitcmp1_b32 s66, 17\n" \
  "s_cbranch_scc1 R3_%=\n" \
  "s_bitcmp1_b32 s68, 3\n" \
  "s_cbranch_scc1 R3_%=\n" \
  "s_sleep 2\n" \
  "s_branch W3_%=\n" \
  "R3_%=:\n" \
  "s_mov_b64 s[64:65], %[hb]\n" \
  "s_add_u32 s64, s64, 6144\n" \
  "s_addc_u32 s65, s65, 0\n" \
  "global_load_dwordx4 v[80:83], %[lo], s[64:65] sc1\n" \
  "global_load_dwordx4 v[84:87], %[lo], s[64:65] offset:1024 sc1\n" \
  "s_sleep 2\n" \
  "s_add_u32 s64, s64, 28672\n" \
  "s_addc_u32 s65, s65, 0\n" \
  "global_load_dwordx4 v[88:91], %[lo], s[64:65] sc1\n" \
  "global_load_dwordx4 v[92:95], %[lo], s[64:65] offset:1024 sc1\n" \
  "s_sleep 2\n" \
  "W4_%=:\n" \
  "s_bitcmp1_b32 s68, 4\n" \
  "s_cbranch_scc1 R4_%=\n" \
  "global_load_dword v12, %[fa], %[fb] sc1\n" \
  "s_waitcnt vmcnt(0)\n" \
  "v_cmp_eq_u32 vcc, %[tg], v12\n" \
  "s_nop 1\n" \
  "s_mov_b32 s68, vcc_lo\n" \
  "s_add_u32 s66, s66, 1\n" \
  "s_bitcmp1_b32 s66, 17\n" \
  "s_cbranch_scc1 R4_%=\n" \
  "s_bitcmp1_b32 s68, 4\n" \
  "s_cbranch_scc1 R4_%=\n" \
  "s_sleep 2\n" \
  "s_branch W4_%=\n" \
  "R4_%=:\n" \
  "s_mov_b64 s[64:65], %[hb]\n" \
  "s_add_u32 s64, s64, 8192\n" \
  "s_addc_u32 s65, s65, 0\n" \
  "global_load_dwordx4 v[96:99], %[lo], s[64:65] sc1\n" \
  "global_load_dwordx4 v[100:103], %[lo], s[64:65] offset:1024 sc1\n" \
  "s_sleep 2\n" \
  "s_add_u32 s64, s64, 28672\n" \
  "s_addc_u32 s65, s65, 0\n" \
  "global_load_dwordx4 v[104:107], %[lo], s[64:65] sc1\n" \
  "global_load_dwordx4 v[108:111], %[lo], s[64:65] offset:1024 sc1\n" \
  "s_sleep 2\n" \
  "W5_%=:\n" \
  "s_bitcmp1_b32 s68, 5\n" \
  "s_cbranch_scc1 R5_%=\n" \
  "global_load_dword v12, %[fa], %[fb] sc1\n" \
  "s_waitcnt vmcnt(0)\n" \
  "v_cmp_eq_u32 vcc, %[tg], v12\n" \
  "s_nop 1\n" \
  "s_mov_b32 s68, vcc_lo\n" \
  "s_add_u32 s66, s66, 1\n" \
  "s_bitcmp1_b32 s66, 17\n" \
  "s_cbranch_scc1 R5_%=\n" \
  "s_bitcmp1_b32 s68, 5\n" \
  "s_cbranch_scc1 R5_%=\n" \
  "s_sleep 2\n" \
  "s_branch W5_%=\n" \
  "R5_%=:\n" \
  "s_mov_b64 s[64:65], %[hb]\n" \
  "s_add_u32 s64, s64, 10240\n" \
  "s_addc_u32 s65, s65, 0\n" \
  "global_load_dwordx4 v[112:115], %[lo], s[64:65] sc1\n" \
  "global_load_dwordx4 v[116:119], %[lo], s[64:65] offset:1024 sc1\n" \
  "s_sleep 2\n" \
  "s_add_u32 s64, s64, 28672\n" \
  "s_addc_u32 s65, s65, 0\n" \
  "global_load_dwordx4 v[120:123], %[lo], s[64:65] sc1\n" \
  "global_load_dwordx4 v[124:127], %[lo], s[64:65] offset:1024 sc1\n" \
  "s_sleep 2\n" \
  "W6_%=:\n" \
  "s_bitcmp1_b32 s68, 6\n" \
  "s_cbranch_scc1 R6_%=\n" \
  "global_load_dword v12, %[fa], %[fb] sc1\n" \
  "s_waitcnt vmcnt(0)\n" \
  "v_cmp_eq_u32 vcc, %[tg], v12\n" \
  "s_nop 1\n" \
  "s_mov_b32 s68, vcc_lo\n" \
  "s_add_u32 s66, s66, 1\n" \
  "s_bitcmp1_b32 s66, 17\n" \
  "s_cbranch_scc1 R6_%=\n" \
  "s_bitcmp1_b32 s68, 6\n" \
  "s_cbranch_scc1 R6_%=\n" \
  "s_sleep 2\n" \
  "s_branch W6_%=\n" \
  "R6_%=:\n" \
  "s_mov_b64 s[64:65], %[hb]\n" \
  "s_add_u32 s64, s64, 12288\n" \
  "s_addc_u32 s65, s65, 0\n" \
  "global_load_dwordx4 v[128:131], %[lo], s[64:65] sc1\n" \
  "global_load_dwordx4 v[132:135], %[lo], s[64:65] offset:1024 sc1\n" \
  "s_sleep 2\n" \
  "s_add_u32 s64, s64, 28672\n" \
  "s_addc_u32 s65, s65, 0\n" \
  "global_load_dwordx4 v[136:139], %[lo], s[64:65] sc1\n" \
  "global_load_dwordx4 v[140:143], %[lo], s[64:65] offset:1024 sc1\n" \
  "s_sleep 2\n" \
  "W7_%=:\n" \
  "s_bitcmp1_b32 s68, 7\n" \
  "s_cbranch_scc1 R7_%=\n" \
  "global_load_dword v12, %[fa], %[fb] sc1\n" \
  "s_waitcnt vmcnt(0)\n" \
  "v_cmp_eq_u32 vcc, %[tg], v12\n" \
  "s_nop 1\n" \
  "s_mov_b32 s68, vcc_lo\n" \
  "s_add_u32 s66, s66, 1\n" \
  "s_bitcmp1_b32 s66, 17\n" \
  "s_cbranch_scc1 R7_%=\n" \
  "s_bitcmp1_b32 s68, 7\n" \
  "s_cbranch_scc1 R7_%=\n" \
  "s_sleep 2\n" \
  "s_branch W7_%=\n" \
  "R7_%=:\n" \
  "s_mov_b64 s[64:65], %[hb]\n" \
  "s_add_u32 s64, s64, 14336\n" \
  "s_addc_u32 s65, s65, 0\n" \
  "global_load_dwordx4 v[144:147], %[lo], s[64:65] sc1\n" \
  "global_load_dwordx4 v[148:151], %[lo], s[64:65] offset:1024 sc1\n" \
  "s_sleep 2\n" \
  "s_add_u32 s64, s64, 28672\n" \
  "s_addc_u32 s65, s65, 0\n" \
  "global_load_dwordx4 v[152:155], %[lo], s[64:65] sc1\n" \
  "global_load_dwordx4 v[156:159], %[lo], s[64:65] offset:1024 sc1\n" \
  "s_sleep 2\n" \
  "W8_%=:\n" \
  "s_bitcmp1_b32 s68, 8\n" \
  "s_cbranch_scc1 R8_%=\n" \
  "global_load_dword v12, %[fa], %[fb] sc1\n" \
  "s_waitcnt vmcnt(0)\n" \
  "v_cmp_eq_u32 vcc, %[tg], v12\n" \
  "s_nop 1\n" \
  "s_mov_b32 s68, vcc_lo\n" \
  "s_add_u32 s66, s66, 1\n" \
  "s_bitcmp1_b32 s66, 17\n" \
  "s_cbranch_scc1 R8_%=\n" \
  "s_bitcmp1_b32 s68, 8\n" \
  "s_cbranch_scc1 R8_%=\n" \
  "s_sleep 2\n" \
  "s_branch W8_%=\n" \
  "R8_%=:\n" \
  "s_mov_b64 s[64:65], %[hb]\n" \
  "s_add_u32 s64, s64, 16384\n" \
  "s_addc_u32 s65, s65, 0\n" \
  "global_load_dwordx4 v[160:163], %[lo], s[64:65] sc1\n" \
  "global_load_dwordx4 v[164:167], %[lo], s[64:65] offset:1024 sc1\n" \
  "s_sleep 2\n" \
  "s_add_u32 s64, s64, 28672\n" \
  "s_addc_u32 s65, s65, 0\n" \
  "global_load_dwordx4 v[168:171], %[lo], s[64:65] sc1\n" \
  "global_load_dwordx4 v[172:175], %[lo], s[64:65] offset:1024 sc1\n" \
  "s_sleep 2\n" \
  "W9_%=:\n" \
  "s_bitcmp1_b32 s68, 9\n" \
  "s_cbranch_scc1 R9_%=\n" \
  "global_load_dword v12, %[fa], %[fb] sc1\n" \
  "s_waitcnt vmcnt(0)\n" \
  "v_cmp_eq_u32 vcc, %[tg], v12\n" \
  "s_nop 1\n" \
  "s_mov_b32 s68, vcc_lo\n" \
  "s_add_u32 s66, s66, 1\n" \
  "s_bitcmp1_b32 s66, 17\n" \
  "s_cbranch_scc1 R9_%=\n" \
  "s_bitcmp1_b32 s68, 9\n" \
  "s_cbranch_scc1 R9_%=\n" \
  "s_sleep 2\n" \
  "s_branch W9_%=\n" \
  "R9_%=:\n" \
  "s_mov_b64 s[64:65], %[hb]\n" \
  "s_add_u32 s64, s64, 18432\n" \
  "s_addc_u32 s65, s65, 0\n" \
  "global_load_dwordx4 v[176:179], %[lo], s[64:65] sc1\n" \
  "global_load_dwordx4 v[180:183], %[lo], s[64:65] offset:1024 sc1\n" \
  "s_sleep 2\n" \
  "s_add_u32 s64, s64, 28672\n" \
  "s_addc_u32 s65, s65, 0\n" \
  "global_load_dwordx4 v[184:187], %[lo], s[64:65] sc1\n" \
  "global_load_dwordx4 v[188:191], %[lo], s[64:65] offset:1024 sc1\n" \
  "s_sleep 2\n" \
  "W10_%=:\n" \
  "s_bitcmp1_b32 s68, 10\n" \
  "s_cbranch_scc1 R10_%=\n" \
  "global_load_dword v12, %[fa], %[fb] sc1\n" \
  "s_waitcnt vmcnt(0)\n" \
  "v_cmp_eq_u32 vcc, %[tg], v12\n" \
  "s_nop 1\n" \
  "s_mov_b32 s68, vcc_lo\n" \
  "s_add_u32 s66, s66, 1\n" \
  "s_bitcmp1_b32 s66, 17\n" \
  "s_cbranch_scc1 R10_%=\n" \
  "s_bitcmp1_b32 s68, 10\n" \
  "s_cbranch_scc1 R10_%=\n" \
  "s_sleep 2\n" \
  "s_branch W10_%=\n" \
  "R10_%=:\n" \
  "s_mov_b64 s[64:65], %[hb]\n" \
  "s_add_u32 s64, s64, 20480\n" \
  "s_addc_u32 s65, s65, 0\n" \
  "global_load_dwordx4 v[192:195], %[lo], s[64:65] sc1\n" \
  "global_load_dwordx4 v[196:199], %[lo], s[64:65] offset:1024 sc1\n" \
  "s_sleep 2\n" \
  "s_add_u32 s64, s64, 28672\n" \
  "s_addc_u32 s65, s65, 0\n" \
  "global_load_dwordx4 v[200:203], %[lo], s[64:65] sc1\n" \
  "global_load_dwordx4 v[204:207], %[lo], s[64:65] offset:1024 sc1\n" \
  "s_sleep 2\n" \
  "W11_%=:\n" \
  "s_bitcmp1_b32 s68, 11\n" \
  "s_cbranch_scc1 R11_%=\n" \
  "global_load_dword v12, %[fa], %[fb] sc1\n" \
  "s_waitcnt vmcnt(0)\n" \
  "v_cmp_eq_u32 vcc, %[tg], v12\n" \
  "s_nop 1\n" \
  "s_mov_b32 s68, vcc_lo\n" \
  "s_add_u32 s66, s66, 1\n" \
  "s_bitcmp1_b32 s66, 17\n" \
  "s_cbranch_scc1 R11_%=\n" \
  "s_bitcmp1_b32 s68, 11\n" \
  "s_cbranch_scc1 R11_%=\n" \
  "s_sleep 2\n" \
  "s_branch W11_%=\n" \
  "R11_%=:\n" \
  "s_mov_b64 s[64:65], %[hb]\n" \
  "s_add_u32 s64, s64, 22528\n" \
  "s_addc_u32 s65, s65, 0\n" \
  "global_load_dwordx4 v[208:211], %[lo], s[64:65] sc1\n" \
  "global_load_dwordx4 v[212:215], %[lo], s[64:65] offset:1024 sc1\n" \
  "s_sleep 2\n" \
  "s_add_u32 s64, s64, 28672\n" \
  "s_addc_u32 s65, s65, 0\n" \
  "global_load_dwordx4 v[216:219], %[lo], s[64:65] sc1\n" \
  "global_load_dwordx4 v[220:223], %[lo], s[64:65] offset:1024 sc1\n" \
  "s_sleep 2\n" \
  "W12_%=:\n" \
  "s_bitcmp1_b32 s68, 12\n" \
  "s_cbranch_scc1 R12_%=\n" \
  "global_load_dword v12, %[fa], %[fb] sc1\n" \
  "s_waitcnt vmcnt(0)\n" \
  "v_cmp_eq_u32 vcc, %[tg], v12\n" \
  "s_nop 1\n" \
  "s_mov_b32 s68, vcc_lo\n" \
  "s_add_u32 s66, s66, 1\n" \
  "s_bitcmp1_b32 s66, 17\n" \
  "s_cbranch_scc1 R12_%=\n" \
  "s_bitcmp1_b32 s68, 12\n" \
  "s_cbranch_scc1 R12_%=\n" \
  "s_sleep 2\n" \
  "s_branch W12_%=\n" \
  "R12_%=:\n" \
  "s_mov_b64 s[64:65], %[hb]\n" \
  "s_add_u32 s64, s64, 24576\n" \
  "s_addc_u32 s65, s65, 0\n" \
  "global_load_dwordx4 v[224:227], %[lo], s[64:65] sc1\n" \
  "global_load_dwordx4 v[228:231], %[lo], s[64:65] offset:1024 sc1\n" \
  "s_sleep 2\n" \
  "s_add_u32 s64, s64, 28672\n" \
  "s_addc_u32 s65, s65, 0\n" \
  "global_load_dwordx4 v[232:235], %[lo], s[64:65] sc1\n" \
  "global_load_dwordx4 v[236:239], %[lo], s[64:65] offset:1024 sc1\n" \
  "s_sleep 2\n" \
  "s_setprio 0\n" \
  "s_mov_b32 s66, 0\n" \
  "YW_%=:\n" \
  "s_sleep 8\n" \
  "ds_read_b32 v12, %[ae]\n" \
  "s_waitcnt lgkmcnt(0)\n" \
  "v_readfirstlane_b32 s67, v12\n" \
  "s_add_u32 s66, s66, 1\n" \
  "s_bitcmp1_b32 s66, 17\n" \
  "s_cbranch_scc1 YG_%=\n" \
  "s_cmp_eq_u32 s67, 0\n" \
  "s_cbranch_scc1 YW_%=\n" \
  "YG_%=:\n" \
  "W13_%=:\n" \
  "s_bitcmp1_b32 s68, 13\n" \
  "s_cbranch_scc1 R13_%=\n" \
  "global_load_dword v12, %[fa], %[fb] sc1\n" \
  "s_waitcnt vmcnt(0)\n" \
  "v_cmp_eq_u32 vcc, %[tg], v12\n" \
  "s_nop 1\n" \
  "s_mov_b32 s68, vcc_lo\n" \
  "s_add_u32 s66, s66, 1\n" \
  "s_bitcmp1_b32 s66, 18\n" \
  "s_cbranch_scc1 R13_%=\n" \
  "s_bitcmp1_b32 s68, 13\n" \
  "s_cbranch_scc1 R13_%=\n" \
  "s_sleep 2\n" \
  "s_branch W13_%=\n" \
  "R13_%=:\n" \
  "ds_read_b128 v[12:15], v8 offset:0\n" \
  "ds_read_b128 v[16:19], v9 offset:0\n" \
  "ds_read_b128 v[20:23], v8 offset:28672\n" \
  "ds_read_b128 v[24:27], v9 offset:28672\n" \
  "ds_read_b128 v[28:31], v10 offset:0\n" \
  "s_waitcnt vmcnt(51) lgkmcnt(4)\n" \
  "v_mfma_f32_32x32x16_bf16 v[240:255], v[32:35], v[12:15], v[240:255]\n" \
  "ds_read_b128 v[12:15], v11 offset:0\n" \
  "s_waitcnt vmcnt(50) lgkmcnt(4)\n" \
  "v_mfma_f32_32x32x16_bf16 v[240:255], v[36:39], v[16:19], v[240:255]\n" \
  "ds_read_b128 v[16:19], v10 offset:28672\n" \
  "s_waitcnt vmcnt(49) lgkmcnt(4)\n" \
  "v_mfma_f32_32x32x16_bf16 v[240:255], v[40:43], v[20:23], v[240:255]\n" \
  "ds_read_b128 v[20:23], v11 offset:28672\n" \
  "s_waitcnt vmcnt(48) lgkmcnt(4)\n" \
  "v_mfma_f32_32x32x16_bf16 v[240:255], v[44:47], v[24:27], v[240:255]\n" \
  "s_mov_b64 s[64:65], %[hb]\n" \
  "s_add_u32 s64, s64, 26624\n" \
  "s_addc_u32 s65, s65, 0\n" \
  "global_load_dwordx4 v[32:35], %[lo], s[64:65] sc1\n" \
  "global_load_dwordx4 v[36:39], %[lo], s[64:65] offset:1024 sc1\n" \
  "s_add_u32 s64, s64, 28672\n" \
  "s_addc_u32 s65, s65, 0\n" \
  "global_load_dwordx4 v[40:43], %[lo], s[64:65] sc1\n" \
  "global_load_dwordx4 v[44:47], %[lo], s[64:65] offset:1024 sc1\n" \
  "ds_read_b128 v[24:27], v8 offset:4096\n" \
  "s_waitcnt vmcnt(51) lgkmcnt(4)\n" \
  "v_mfma_f32_32x32x16_bf16 v[240:255], v[48:51], v[28:31], v[240:255]\n" \
  "ds_read_b128 v[28:31], v9 offset:4096\n" \
  "s_waitcnt vmcnt(50) lgkmcnt(4)\n" \
  "v_mfma_f32_32x32x16_bf16 v[240:255], v[52:55], v[12:15], v[240:255]\n" \
  "ds_read_b128 v[12:15], v8 offset:32768\n" \
  "s_waitcnt vmcnt(49) lgkmcnt(4)\n" \
  "v_mfma_f32_32x32x16_bf16 v[240:255], v[56:59], v[16:19], v[240:255]\n" \
  "ds_read_b128 v[16:19], v9 offset:32768\n" \
  "s_waitcnt vmcnt(48) lgkmcnt(4)\n" \
  "v_mfma_f32_32x32x16_bf16 v[240:255], v[60:63], v[20:23], v[240:255]\n" \
  "ds_read_b128 v[20:23], v10 offset:4096\n" \
  "s_waitcnt vmcnt(47) lgkmcnt(4)\n" \
  "v_mfma_f32_32x32x16_bf16 v[240:255], v[64:67], v[24:27], v[240:255]\n" \
  "ds_read_b128 v[24:27], v11 offset:4096\n" \
  "s_waitcnt vmcnt(46) lgkmcnt(4)\n" \
  "v_mfma_f32_32x32x16_bf16 v[240:255], v[68:71], v[28:31], v[240:255]\n" \
  "ds_read_b128 v[28:31], v10 offset:32768\n" \
  "s_waitcnt vmcnt(45) lgkmcnt(4)\n" \
  "v_mfma_f32_32x32x16_bf16 v[240:255], v[72:75], v[12:15], v[240:255]\n" \
  "ds_read_b128 v[12:15], v11 offset:32768\n" \
  "s_waitcnt vmcnt(44) lgkmcnt(4)\n" \
  "v_mfma_f32_32x32x16_bf16 v[240:255], v[76:79], v[16:19], v[240:255]\n" \
  "ds_read_b128 v[16:19], v8 offset:8192\n" \
  "s_waitcnt vmcnt(43) lgkmcnt(4)\n" \
  "v_mfma_f32_32x32x16_bf16 v[240:255], v[80:83], v[20:23], v[240:255]\n" \
  "ds_read_b128 v[20:23], v9 offset:8192\n" \
  "s_waitcnt vmcnt(42) lgkmcnt(4)\n" \
  "v_mfma_f32_32x32x16_bf16 v[240:255], v[84:87], v[24:27], v[240:255]\n" \
  "ds_read_b128 v[24:27], v8 offset:36864\n" \
  "s_waitcnt vmcnt(41) lgkmcnt(4)\n" \
  "v_mfma_f32_32x32x16_bf16 v[240:255], v[88:91], v[28:31], v[240:255]\n" \
  "ds_read_b128 v[28:31], v9 offset:36864\n" \
  "s_waitcnt vmcnt(40) lgkmcnt(4)\n" \
  "v_mfma_f32_32x32x16_bf16 v[240:255], v[92:95], v[12:15], v[240:255]\n" \
  "ds_read_b128 v[12:15], v10 offset:8192\n" \
  "s_waitcnt vmcnt(39) lgkmcnt(4)\n" \
  "v_mfma_f32_32x32x16_bf16 v[240:255], v[96:99], v[16:19], v[240:255]\n" \
  "ds_read_b128 v[16:19], v11 offset:8192\n" \
  "s_waitcnt vmcnt(38) lgkmcnt(4)\n" \
  "v_mfma_f32_32x32x16_bf16 v[240:255], v[100:103], v[20:23], v[240:255]\n" \
  "ds_read_b128 v[20:23], v10 offset:36864\n" \
  "s_waitcnt vmcnt(37) lgkmcnt(4)\n" \
  "v_mfma_f32_32x32x16_bf16 v[240:255], v[104:107], v[24:27], v[240:255]\n" \
  "ds_read_b128 v[24:27], v11 offset:36864\n" \
  "s_waitcnt vmcnt(36) lgkmcnt(4)\n" \
  "v_mfma_f32_32x32x16_bf16 v[240:255], v[108:111], v[28:31], v[240:255]\n" \
  "ds_read_b128 v[28:31], v8 offset:12288\n" \
  "s_waitcnt vmcnt(35) lgkmcnt(4)\n" \
  "v_mfma_f32_32x32x16_bf16 v[240:255], v[112:115], v[12:15], v[240:255]\n" \
  "ds_read_b128 v[12:15], v9 offset:12288\n" \
  "s_waitcnt vmcnt(34) lgkmcnt(4)\n" \
  "v_mfma_f32_32x32x16_bf16 v[240:255], v[116:119], v[16:19], v[240:255]\n" \
  "ds_read_b128 v[16:19], v8 offset:40960\n" \
  "s_waitcnt vmcnt(33) lgkmcnt(4)\n" \
  "v_mfma_f32_32x32x16_bf16 v[240:255], v[120:123], v[20:23], v[240:255]\n" \
  "ds_read_b128 v[20:23], v9 offset:40960\n" \
  "s_waitcnt vmcnt(32) lgkmcnt(4)\n" \
  "v_mfma_f32_32x32x16_bf16 v[240:255], v[124:127], v[24:27], v[240:255]\n" \
  "ds_read_b128 v[24:27], v10 offset:12288\n" \
  "s_waitcnt vmcnt(31) lgkmcnt(4)\n" \
  "v_mfma_f32_32x32x16_bf16 v[240:255], v[128:131], v[28:31], v[240:255]\n" \
  "ds_read_b128 v[28:31], v11 offset:12288\n" \
  "s_waitcnt vmcnt(30) lgkmcnt(4)\n" \
  "v_mfma_f32_32x32x16_bf16 v[240:255], v[132:135], v[12:15], v[240:255]\n" \
  "ds_read_b128 v[12:15], v10 offset:40960\n" \
  "s_waitcnt vmcnt(29) lgkmcnt(4)\n" \
  "v_mfma_f32_32x32x16_bf16 v[240:255], v[136:139], v[16:19], v[240:255]\n" \
  "ds_read_b128 v[16:19], v11 offset:40960\n" \
  "s_waitcnt vmcnt(28) lgkmcnt(4)\n" \
  "v_mfma_f32_32x32x16_bf16 v[240:255], v[140:143], v[20:23], v[240:255]\n" \
  "ds_read_b128 v[20:23], v8 offset:16384\n" \
  "s_waitcnt vmcnt(27) lgkmcnt(4)\n" \
  "v_mfma_f32_32x32x16_bf16 v[240:255], v[144:147], v[24:27], v[240:255]\n" \
  "ds_read_b128 v[24:27], v9 offset:16384\n" \
  "s_waitcnt vmcnt(26) lgkmcnt(4)\n" \
  "v_mfma_f32_32x32x16_bf16 v[240:255], v[148:151], v[28:31], v[240:255]\n" \
  "ds_read_b128 v[28:31], v8 offset:45056\n" \
  "s_waitcnt vmcnt(25) lgkmcnt(4)\n" \
  "v_mfma_f32_32x32x16_bf16 v[240:255], v[152:155], v[12:15], v[240:255]\n" \
  "ds_read_b128 v[12:15], v9 offset:45056\n" \
  "s_waitcnt vmcnt(24) lgkmcnt(4)\n" \
  "v_mfma_f32_32x32x16_bf16 v[240:255], v[156:159], v[16:19], v[240:255]\n" \
  "ds_read_b128 v[16:19], v10 offset:16384\n" \
  "s_waitcnt vmcnt(23) lgkmcnt(4)\n" \
  "v_mfma_f32_32x32x16_bf16 v[240:255], v[160:163], v[20:23], v[240:255]\n" \
  "ds_read_b128 v[20:23], v11 offset:16384\n" \
  "s_waitcnt vmcnt(22) lgkmcnt(4)\n" \
  "v_mfma_f32_32x32x16_bf16 v[240:255], v[164:167], v[24:27], v[240:255]\n" \
  "ds_read_b128 v[24:27], v10 offset:45056\n" \
  "s_waitcnt vmcnt(21) lgkmcnt(4)\n" \
  "v_mfma_f32_32x32x16_bf16 v[240:255], v[168:171], v[28:31], v[240:255]\n" \
  "ds_read_b128 v[28:31], v11 offset:45056\n" \
  "s_waitcnt vmcnt(20) lgkmcnt(4)\n" \
  "v_mfma_f32_32x32x16_bf16 v[240:255], v[172:175], v[12:15], v[240:255]\n" \
  "ds_read_b128 v[12:15], v8 offset:20480\n" \
  "s_waitcnt vmcnt(19) lgkmcnt(4)\n" \
  "v_mfma_f32_32x32x16_bf16 v[240:255], v[176:179], v[16:19], v[240:255]\n" \
  "ds_read_b128 v[16:19], v9 offset:20480\n" \
  "s_waitcnt vmcnt(18) lgkmcnt(4)\n" \
  "v_mfma_f32_32x32x16_bf16 v[240:255], v[180:183], v[20:23], v[240:255]\n" \
  "ds_read_b128 v[20:23], v8 offset:49152\n" \
  "s_waitcnt vmcnt(17) lgkmcnt(4)\n" \
  "v_mfma_f32_32x32x16_bf16 v[240:255], v[184:187], v[24:27], v[240:255]\n" \
  "ds_read_b128 v[24:27], v9 offset:49152\n" \
  "s_waitcnt vmcnt(16) lgkmcnt(4)\n" \
  "v_mfma_f32_32x32x16_bf16 v[240:255], v[188:191], v[28:31], v[240:255]\n" \
  "ds_read_b128 v[28:31], v10 offset:20480\n" \
  "s_waitcnt vmcnt(15) lgkmcnt(4)\n" \
  "v_mfma_f32_32x32x16_bf16 v[240:255], v[192:195], v[12:15], v[240:255]\n" \
  "ds_read_b128 v[12:15], v11 offset:20480\n" \
  "s_waitcnt vmcnt(14) lgkmcnt(4)\n" \
  "v_mfma_f32_32x32x16_bf16 v[240:255], v[196:199], v[16:19], v[240:255]\n" \
  "ds_read_b128 v[16:19], v10 offset:49152\n" \
  "s_waitcnt vmcnt(13) lgkmcnt(4)\n" \
  "v_mfma_f32_32x32x16_bf16 v[240:255], v[200:203], v[20:23], v[240:255]\n" \
  "ds_read_b128 v[20:23], v11 offset:49152\n" \
  "s_waitcnt vmcnt(12) lgkmcnt(4)\n" \
  "v_mfma_f32_32x32x16_bf16 v[240:255], v[204:207], v[24:27], v[240:255]\n" \
  "ds_read_b128 v[24:27], v8 offset:24576\n" \
  "s_waitcnt vmcnt(11) lgkmcnt(4)\n" \
  "v_mfma_f32_32x32x16_bf16 v[240:255], v[208:211], v[28:31], v[240:255]\n" \
  "ds_read_b128 v[28:31], v9 offset:24576\n" \
  "s_waitcnt vmcnt(10) lgkmcnt(4)\n" \
  "v_mfma_f32_32x32x16_bf16 v[240:255], v[212:215], v[12:15], v[240:255]\n" \
  "ds_read_b128 v[12:15], v8 offset:53248\n" \
  "s_waitcnt vmcnt(9) lgkmcnt(4)\n" \
  "v_mfma_f32_32x32x16_bf16 v[240:255], v[216:219], v[16:19], v[240:255]\n" \
  "ds_read_b128 v[16:19], v9 offset:53248\n" \
  "s_waitcnt vmcnt(8) lgkmcnt(4)\n" \
  "v_mfma_f32_32x32x16_bf16 v[240:255], v[220:223], v[20:23], v[240:255]\n" \
  "ds_read_b128 v[20:23], v10 offset:24576\n" \
  "s_waitcnt vmcnt(7) lgkmcnt(4)\n" \
  "v_mfma_f32_32x32x16_bf16 v[240:255], v[224:227], v[24:27], v[240:255]\n" \
  "ds_read_b128 v[24:27], v11 offset:24576\n" \
  "s_waitcnt vmcnt(6) lgkmcnt(4)\n" \
  "v_mfma_f32_32x32x16_bf16 v[240:255], v[228:231], v[28:31], v[240:255]\n" \
  "ds_read_b128 v[28:31], v10 offset:53248\n" \
  "s_waitcnt vmcnt(5) lgkmcnt(4)\n" \
  "v_mfma_f32_32x32x16_bf16 v[240:255], v[232:235], v[12:15], v[240:255]\n" \
  "ds_read_b128 v[12:15], v11 offset:53248\n" \
  "s_waitcnt vmcnt(4) lgkmcnt(4)\n" \
  "v_mfma_f32_32x32x16_bf16 v[240:255], v[236:239], v[16:19], v[240:255]\n" \
  "s_waitcnt vmcnt(3) lgkmcnt(3)\n" \
  "v_mfma_f32_32x32x16_bf16 v[240:255], v[32:35], v[20:23], v[240:255]\n" \
  "s_waitcnt vmcnt(2) lgkmcnt(2)\n" \
  "v_mfma_f32_32x32x16_bf16 v[240:255], v[36:39], v[24:27], v[240:255]\n" \
  "s_waitcnt vmcnt(1) lgkmcnt(1)\n" \
  "v_mfma_f32_32x32x16_bf16 v[240:255], v[40:43], v[28:31], v[240:255]\n" \
  "s_waitcnt vmcnt(0) lgkmcnt(0)\n" \
  "v_mfma_f32_32x32x16_bf16 v[240:255], v[44:47], v[12:15], v[240:255]\n" \
  "s_nop 15\n"
#define Y_CLOBBERS "v8", "v9", "v10", "v11", "v12", "v13", "v14", "v15", "v16", "v17", "v18", "v19", "v20", "v21", "v22", "v23", "v24", "v25", "v26", "v27", "v28", "v29", "v30", "v31", "v32", "v33", "v34", "v35", "v36", "v37", "v38", "v39", "v40", "v41", "v42", "v43", "v44", "v45", "v46", "v47", "v48", "v49", "v50", "v51", "v52", "v53", "v54", "v55", "v56", "v57", "v58", "v59", "v60", "v61", "v62", "v63", "v64", "v65", "v66", "v67", "v68", "v69", "v70", "v71", "v72", "v73", "v74", "v75", "v76", "v77", "v78", "v79", "v80", "v81", "v82", "v83", "v84", "v85", "v86", "v87", "v88", "v89", "v90", "v91", "v92", "v93", "v94", "v95", "v96", "v97", "v98", "v99", "v100", "v101", "v102", "v103", "v104", "v105", "v106", "v107", "v108", "v109", "v110", "v111", "v112", "v113", "v114", "v115", "v116", "v117", "v118", "v119", "v120", "v121", "v122", "v123", "v124", "v125", "v126", "v127", "v128", "v129", "v130", "v131", "v132", "v133", "v134", "v135", "v136", "v137", "v138", "v139", "v140", "v141", "v142", "v143", "v144", "v145", "v146", "v147", "v148", "v149", "v150", "v151", "v152", "v153", "v154", "v155", "v156", "v157", "v158", "v159", "v160", "v161", "v162", "v163", "v164", "v165", "v166", "v167", "v168", "v169", "v170", "v171", "v172", "v173", "v174", "v175", "v176", "v177", "v178", "v179", "v180", "v181", "v182", "v183", "v184", "v185", "v186", "v187", "v188", "v189", "v190", "v191", "v192", "v193", "v194", "v195", "v196", "v197", "v198", "v199", "v200", "v201", "v202", "v203", "v204", "v205", "v206", "v207", "v208", "v209", "v210", "v211", "v212", "v213", "v214", "v215", "v216", "v217", "v218", "v219", "v220", "v221", "v222", "v223", "v224", "v225", "v226", "v227", "v228", "v229", "v230", "v231", "v232", "v233", "v234", "v235", "v236", "v237", "v238", "v239", "s64", "s65", "s66", "s67", "s68", "vcc", "scc", "memory"

typedef short bf16x8 __attribute__((ext_vector_type(8)));
typedef short s16x4 __attribute__((ext_vector_type(4)));
typedef float f32x16 __attribute__((ext_vector_type(16)));
typedef float f32x4 __attribute__((ext_vector_type(4)));
typedef float f32x2 __attribute__((ext_vector_type(2)));
typedef unsigned u32x4 __attribute__((ext_vector_type(4)));
typedef unsigned u32x2 __attribute__((ext_vector_type(2)));
typedef __bf16 bf2 __attribute__((ext_vector_type(2)));
#define LDSA __attribute__((address_space(3)))
#define GAS __attribute__((address_space(1)))
#define CAS __attribute__((address_space(4)))
#define DI __device__ __forceinline__
#define MFMA(a, b, c) __builtin_amdgcn_mfma_f32_32x32x16_bf16((a), (b), (c), 0, 0, 0)
#define SBAR() __builtin_amdgcn_sched_barrier(0)

constexpr unsigned WS_KF = 2u << 20, WS_VF = 4u << 20, WS_OF = 6u << 20, WS_HF = 8u << 20, WS_CTL = 12u << 20, WS_USED = 13u << 20;
constexpr unsigned FST = 32;
constexpr unsigned CT_GEN = WS_CTL, CT_F1 = WS_CTL + 4096, CT_F2 = CT_F1 + 32768, CT_F3 = CT_F2 + 32768, CT_START = CT_F3 + 32768, CT_EG = CT_START + 4096;
constexpr int QKV_SLOT = 28672, BU_SLOT = 24576;
constexpr int L_BU = 58880;
constexpr int L_XCH = 57856, L_S = 75264, L_SX = 157184, L_DUMMY = L_SX + 4096, L_CTL = 163584, LDS_BYTES = 163840;
constexpr unsigned SPIN_CAP = 1u << 17, LSPIN_CAP = 1u << 18;

#if defined(__HIP_DEVICE_COMPILE__)
#pragma clang attribute push(__attribute__((target("no-packed-fp32-ops"))), apply_to = function)
#endif

DI unsigned pk(float lo, float hi) { f32x2 v = {lo, hi}; bf2 r = __builtin_convertvector(v, bf2); return __builtin_bit_cast(unsigned, r); }
DI float bf2f(unsigned b) { return __builtin_bit_cast(float, b << 16); }
DI float asf(unsigned u) { return __builtin_bit_cast(float, u); }
DI unsigned asu(float f) { return __builtin_bit_cast(unsigned, f); }
DI bf16x8 asfrag(u32x4 v) { return __builtin_bit_cast(bf16x8, v); }
DI void glds16(const void* sbase, unsigned voff, unsigned lds_base) {
    unsigned sv;
    asm volatile("s_mov_b32 %0, m0\n\ts_mov_b32 m0, %3\n\ts_nop 0\n\tglobal_load_lds_dwordx4 %1, %2\n\ts_mov_b32 m0, %0" : "=&s"(sv) : "v"(voff), "s"(sbase), "s"(lds_base) : "memory");
}
#define WAITVM(N) asm volatile("s_waitcnt vmcnt(" #N ")" ::: "memory")
#define WAITLGKM() asm volatile("s_waitcnt lgkmcnt(0)" ::: "memory")
#define HWBAR() asm volatile("s_waitcnt lgkmcnt(0)\n\ts_barrier" ::: "memory")
DI s16x4 trread(LDSA const char* p) { return __builtin_bit_cast(s16x4, __builtin_amdgcn_ds_read_tr16_b64_v4i16((LDSA s16x4*)p)); }
DI bf16x8 cat(s16x4 lo, s16x4 hi) { return __builtin_shufflevector(lo, hi, 0, 1, 2, 3, 4, 5, 6, 7); }
DI bf16x8 ldsb128(LDSA const char* p) { return *(LDSA const bf16x8*)p; }
DI float lohalf(float v) { return asf(__builtin_amdgcn_permlane32_swap(asu(v), asu(v), false, false)[0]); }
DI float hihalf(float v) { return asf(__builtin_amdgcn_permlane32_swap(asu(v), asu(v), false, false)[1]); }
DI bool anylane(bool c) { return __builtin_amdgcn_ballot_w64(c) != 0ull; }
#define RLX_AGENT __ATOMIC_RELAXED, __HIP_MEMORY_SCOPE_AGENT
#define RLX_WG __ATOMIC_RELAXED, __HIP_MEMORY_SCOPE_WORKGROUP
typedef __amdgpu_buffer_rsrc_t rsrc_t;
DI u32x4 ld_sc1(rsrc_t r, unsigned off) { return __builtin_amdgcn_raw_buffer_load_b128(r, off, 0, 16); }
DI void st_sc1(rsrc_t r, unsigned off, u32x4 v) { __builtin_amdgcn_raw_buffer_store_b128(v, r, off, 0, 16); }

template <int SLEEP = 0>
DI void rolebar(LDSA unsigned* cnt, unsigned& target, int lane) {
    target += 4;
    WAITLGKM();
    if (lane == 0) __hip_atomic_fetch_add(cnt, 1u, RLX_WG);
#pragma unroll 1
    for (unsigned spins = 0; spins < LSPIN_CAP && __builtin_amdgcn_readfirstlane(__hip_atomic_load(cnt, RLX_WG)) < target; ++spins) { if (SLEEP) __builtin_amdgcn_s_sleep(SLEEP); }
    asm volatile("" ::: "memory");
}
DI void ldswait(LDSA unsigned* w, unsigned target) {
#pragma unroll 1
    for (unsigned spins = 0; spins < LSPIN_CAP && __builtin_amdgcn_readfirstlane(__hip_atomic_load(w, RLX_WG)) < target; ++spins) __builtin_amdgcn_s_sleep(4);
    asm volatile("" ::: "memory");
}
DI void ldswait4(LDSA unsigned* w, unsigned target) {
#pragma unroll 1
    for (unsigned spins = 0; spins < LSPIN_CAP; ++spins) {
        const u32x4 v = *(volatile LDSA u32x4*)w;
        const unsigned m = __builtin_elementwise_min(__builtin_elementwise_min(v[0], v[1]), __builtin_elementwise_min(v[2], v[3]));
        if (__builtin_amdgcn_readfirstlane(m) >= target) break;
        __builtin_amdgcn_s_sleep(2);
    }
    asm volatile("" ::: "memory");
}
DI void pollflags(char* ws, unsigned off, bool active, unsigned tag) {
    GAS unsigned* p = (GAS unsigned*)(ws + off);
#pragma unroll 1
    for (unsigned spins = 0; spins < SPIN_CAP; ++spins) {
        const unsigned v = active ? __hip_atomic_load(p, RLX_AGENT) : tag;
        if (!anylane(v != tag)) break;
        __builtin_amdgcn_s_sleep(1);
    }
    asm volatile("" ::: "memory");
}

DI void store_tile(rsrc_t rout, unsigned byte0, const f32x16& c, LDSA char* scr, int lane) {
    const int r = lane & 31, hh = lane >> 5;
#pragma unroll
    for (int j = 0; j < 16; ++j) *(LDSA float*)(scr + ((j & 3) + 8 * (j >> 2) + 4 * hh) * 128 + r * 4) = c[j];
    WAITLGKM();
    u32x4 t[4];
#pragma unroll
    for (int i = 0; i < 4; ++i) t[i] = *(LDSA const u32x4*)(scr + (8 * i + (lane >> 3)) * 128 + (lane & 7) * 16);
#pragma unroll
    for (int i = 0; i < 4; ++i) __builtin_amdgcn_raw_buffer_store_b128(t[i], rout, byte0 + (unsigned)(8 * i + (lane >> 3)) * 3584 + (lane & 7) * 16, 0, 16);
}
struct cf { float re, im; };
DI cf cmul(cf a, cf b) { return {a.re * b.re - a.im * b.im, a.re * b.im + a.im * b.re}; }
DI cf cfma(cf a, cf b, cf c) { return {__builtin_fmaf(a.re, b.re, __builtin_fmaf(-a.im, b.im, c.re)), __builtin_fmaf(a.re, b.im, __builtin_fmaf(a.im, b.re, c.im))}; }

struct Params {
    const char *xA; const unsigned short *Lre, *Lim; const char *Bre, *Bim, *Cre, *Cim; const unsigned short *ldt, *Dsk; const char *xB, *wqkv, *wo; float* out; char* ws;
};

__global__ __launch_bounds__(512, 2) void fused(Params P) {
    extern __shared__ __attribute__((aligned(16))) char smem[];
    const int tid = __builtin_amdgcn_workitem_id_x(), lane = tid & 63, wave = __builtin_amdgcn_readfirstlane(tid >> 6);
    const int b = __builtin_amdgcn_workgroup_id_x(), xcd = b & 7, jx = b >> 3;
    const int wv = wave & 3, r = lane & 31, hh = lane >> 5;
    LDSA char* sm = (LDSA char*)smem;
    const unsigned lds0 = (unsigned)(uintptr_t)smem;
    SBAR();
    asm volatile("" :: "s"(P.xA), "s"(P.Lre), "s"(P.Lim), "s"(P.Bre), "s"(P.Bim), "s"(P.Cre), "s"(P.Cim), "s"(P.ldt), "s"(P.Dsk), "s"(P.xB), "s"(P.wqkv), "s"(P.wo), "s"(P.out), "s"(P.ws));
    char* const ws = P.ws;
    const rsrc_t rws = __builtin_amdgcn_make_buffer_rsrc(ws, (short)0, (int)WS_USED, 0x00020000);
    LDSA unsigned* const barA = (LDSA unsigned*)(sm + L_CTL), * const barS = barA + 16, * const qfull = barA + 32, * const qfree = barA + 36, * const tagw = barA + 40;
    if (tid < 48) barA[tid] = 0u;
    unsigned bart = 0, tag;

    if (wave < 4) {
        const int rt = (xcd & 3) * 4 + (jx & 3), g = (xcd >> 2) * 8 + (jx >> 2);
        u32x4 kown0, kown1, vown0, vown1;
        {
            HWBAR();
            if (wave == 0) {
                const unsigned t = *(CAS const volatile unsigned*)(ws + CT_GEN) + 1u;
                if (lane == 0) { __hip_atomic_store(tagw + 1, t, RLX_WG); __hip_atomic_store(tagw, 1u, RLX_WG); __hip_atomic_store((GAS unsigned*)(ws + CT_START) + b, t, RLX_AGENT); }
            }
            if (wave == 1 && jx < 28) {
                const int p = (7 * (xcd >> 2) + (jx >> 2)) * 32 + r;
                const float lr = bf2f(P.Lre[p]), li = bf2f(P.Lim[p]);
                float dt = __builtin_amdgcn_exp2f(bf2f(P.ldt[p]) * 1.4426950408889634f);
                dt = bf2f(pk(dt, 0.f) & 0xffffu);
                const float ea = __builtin_amdgcn_exp2f(dt * lr * 1.4426950408889634f);
                const float rev = __builtin_amdgcn_fractf(dt * li * 0.15915494309189535f);
                const cf lam = {ea * __builtin_amdgcn_cosf(rev), ea * __builtin_amdgcn_sinf(rev)};
                const float nr = lam.re - 1.0f, ni = lam.im, rden = 2.0f * __builtin_amdgcn_rcpf(lr * lr + li * li);
                const cf c2 = {(nr * lr + ni * li) * rden, (ni * lr - nr * li) * rden};
                const cf l2 = cmul(lam, lam), l4 = cmul(l2, l2), l8 = cmul(l4, l4), l16 = cmul(l8, l8), l32 = cmul(l16, l16), l64 = cmul(l32, l32), l128 = cmul(l64, l64);
                LDSA f32x2* co = (LDSA f32x2*)(sm + L_SX + 1536) + r;
                if (hh == 0) { co[0] = (f32x2){lam.re, lam.im}; co[32] = (f32x2){l16.re, l16.im}; co[64] = (f32x2){l32.re, l32.im}; co[96] = (f32x2){l128.re, l128.im}; co[128] = (f32x2){c2.re, c2.im}; }
                WAITLGKM();
                if (lane == 0) __hip_atomic_store(tagw + 2, 1u, RLX_WG);
            }
            const int q4 = (lane & 15) >> 2, cb = 32 * ((lane >> 4) & 1) + 8 * (lane & 3);
            const unsigned xa = lds0 + (4 * wv + (r >> 3)) * 1024 + (r & 7) * 128 + ((hh ^ ((r >> 1) & 7)) << 4);
            const unsigned wq = lds0 + 16384 + (8 * hh + q4) * 64 + cb;
            f32x16 acc0, acc1, acc2;
            asm volatile(QKV_ASM_QKV : "=&{v[16:31]}"(acc0), "=&{v[32:47]}"(acc1), "=&{v[48:63]}"(acc2)
                : [x0] "v"(xa), [x1] "v"(xa ^ 32u), [x2] "v"(xa ^ 64u), [x3] "v"(xa ^ 96u), [w0] "v"(wq), [w1] "v"(wq + 4096u), [w2] "v"(wq + 8192u + (g == 15 ? 8u : 0u)),
                  [ct] "v"(lds0 + L_CTL + 128u), [fr] "v"(lds0 + L_CTL + 144u + wv * 4) : QKV_CLOBBERS);
            ldswait(tagw, 1u);
            tag = __builtin_amdgcn_readfirstlane(__hip_atomic_load(tagw + 1, RLX_WG));
            {
                kown0 = (u32x4){pk(acc1[0], acc1[1]), pk(acc1[2], acc1[3]), pk(acc1[4], acc1[5]), pk(acc1[6], acc1[7])};
                kown1 = (u32x4){pk(acc1[8], acc1[9]), pk(acc1[10], acc1[11]), pk(acc1[12], acc1[13]), pk(acc1[14], acc1[15])};
                if (hh) { kown1[2] = 0x3f803f80u; kown1[3] = 0x00003f80u; }
                vown0 = (u32x4){pk(acc2[0], acc2[1]), pk(acc2[2], acc2[3]), pk(acc2[4], acc2[5]), pk(acc2[6], acc2[7])};
                vown1 = (u32x4){pk(acc2[8], acc2[9]), pk(acc2[10], acc2[11]), pk(acc2[12], acc2[13]), pk(acc2[14], acc2[15])};
                if (r >= 28) { const unsigned cst = r == 28 ? 0x3f803f80u : 0u; vown0 = (u32x4){cst, cst, cst, cst}; vown1 = vown0; }
                const unsigned dst = (unsigned)(g * 64 + rt * 4 + wv) * 2048 + lane * 16;
                st_sc1(rws, WS_KF + dst, kown0); st_sc1(rws, WS_KF + dst + 1024, kown1); st_sc1(rws, WS_VF + dst, vown0); st_sc1(rws, WS_VF + dst + 1024, vown1);
            }
            {
                const float sc = 0.18898223650461362f * 1.4426950408889634f;
#pragma unroll
                for (int j = 0; j < 16; ++j) acc0[j] *= sc;
                const u32x4 f0 = {pk(acc0[0], acc0[1]), pk(acc0[2], acc0[3]), pk(acc0[4], acc0[5]), pk(acc0[6], acc0[7])};
                u32x4 f1 = {pk(acc0[8], acc0[9]), pk(acc0[10], acc0[11]), pk(acc0[12], acc0[13]), pk(acc0[14], acc0[15])};
                if (hh) { f1[2] = 0u; f1[3] = 0u; }
                *(LDSA u32x4*)(sm + wv * 4096 + lane * 16) = f0; *(LDSA u32x4*)(sm + wv * 4096 + 1024 + lane * 16) = f1;
                if (wv == 0) { *(LDSA u32x4*)(sm + 2048 + lane * 16) = kown0; *(LDSA u32x4*)(sm + 3072 + lane * 16) = kown1; }
            }
            rolebar(barA, bart, lane);
        }
        const int head = g, qt = rt, rot = qt * 4;
        f32x16 o; u32x4 qown0, qown1;
        {
            unsigned ko = __builtin_amdgcn_readfirstlane((unsigned)(((rot + wv) * 2048 - 8192) & 0x1ffff)), vo = ko, cnt;
            const char* kb = ws + WS_KF + (size_t)head * 131072; const char* vb = ws + WS_VF + (size_t)head * 131072;
            const unsigned rb = lds0 + lane * 16;
            asm volatile(ATTN_LOOP_ASM
                : "=&{v[96:111]}"(o), "=&{v[40:43]}"(qown0), "=&{v[44:47]}"(qown1), "+{v[160:163]}"(kown0), "+{v[164:167]}"(kown1), "+{v[192:195]}"(vown0), "+{v[196:199]}"(vown1), [ko] "+&s"(ko), [vo] "+&s"(vo), [cnt] "=&s"(cnt)
                : [kb] "s"(kb), [vb] "s"(vb), [wv] "s"(wv), [lo] "v"(lane * 16), [rb] "v"(rb), [fa] "v"(CT_F1 + ((head * 4 + wv) * 16 + (lane & 15)) * FST), [fm] "v"(CT_F1 + ((head * 4 + wv) * 16 + qt) * FST), [fb] "s"(ws), [tg] "s"(tag),
                  [am] "s"((unsigned)((((0x1eu << qt) | (0x1eu << qt >> 16)) & 0xffffu))),
                  [r0] "v"(rb + ((wv + 1) & 3) * 12288 + 8192), [r1] "v"(rb + ((wv + 2) & 3) * 12288 + 4096), [r2] "v"(rb + ((wv + 3) & 3) * 12288)
                : ATTN_LOOP_CLOBBERS);
        }
        __hip_atomic_store(tagw + 5, 1u, RLX_WG);
        rolebar(barA, bart, lane);
        {
            LDSA const f32x4* red = (LDSA const f32x4*)(sm + wv * 12288);
            f32x4 t[12];
#pragma unroll
            for (int i = 0; i < 12; ++i) t[i] = red[i * 64 + lane];
            SBAR();
#pragma unroll
            for (int i = 0; i < 12; ++i) { const int j = i & 3; o[4 * j] += t[i][0]; o[4 * j + 1] += t[i][1]; o[4 * j + 2] += t[i][2]; o[4 * j + 3] += t[i][3]; }
        }
        {
            float amax = 0.f;
#pragma unroll
            for (int j = 0; j < 16; ++j) amax = __builtin_fmaxf(amax, __builtin_fabsf(o[j]));
            bool ok = amax < 1e37f;
            if (hh) ok = ok && (o[12] >= 0.25f) && (o[12] <= 1.8e19f);
            if (anylane(!ok)) {
                const unsigned koff = WS_KF + (unsigned)(head * 64) * 2048 + lane * 16;
                const u32x4 q0 = qown0; u32x4 q1 = qown1;
                if (hh) { q1[2] = 0u; q1[3] = 0u; }
                const f32x16 zero = {};
                float M = -__builtin_inff();
#pragma unroll 1
                for (int kt = 0; kt < 64; ++kt) {
                    f32x16 s = MFMA(asfrag(ld_sc1(rws, koff + kt * 2048)), asfrag(q0), zero); s = MFMA(asfrag(ld_sc1(rws, koff + kt * 2048 + 1024)), asfrag(q1), s);
#pragma unroll
                    for (int j = 0; j < 16; ++j) M = __builtin_fmaxf(M, s[j]);
                }
                M = __builtin_fmaxf(lohalf(M), hihalf(M));
                const unsigned hi = pk(M, 0.f) & 0xffffu; const float r1 = M - bf2f(hi);
                const unsigned mid = pk(r1, 0.f) & 0xffffu; const float r2 = r1 - bf2f(mid);
                const unsigned lo = pk(r2, 0.f) & 0xffffu;
                if (hh) { q1[2] = (hi ^ 0x8000u) | ((mid ^ 0x8000u) << 16); q1[3] = lo ^ 0x8000u; }
                o = zero;
#pragma unroll 1
                for (int kt = 0; kt < 64; ++kt) {
                    f32x16 s = MFMA(asfrag(ld_sc1(rws, koff + kt * 2048)), asfrag(q0), zero); s = MFMA(asfrag(ld_sc1(rws, koff + kt * 2048 + 1024)), asfrag(q1), s);
#pragma unroll
                    for (int j = 0; j < 16; ++j) s[j] = __builtin_amdgcn_exp2f(s[j]);
                    const u32x4 p0 = {pk(s[0], s[1]), pk(s[2], s[3]), pk(s[4], s[5]), pk(s[6], s[7])}, p1 = {pk(s[8], s[9]), pk(s[10], s[11]), pk(s[12], s[13]), pk(s[14], s[15])};
                    o = MFMA(asfrag(ld_sc1(rws, koff + (2u << 20) + kt * 2048)), asfrag(p0), o); o = MFMA(asfrag(ld_sc1(rws, koff + (2u << 20) + kt * 2048 + 1024)), asfrag(p1), o);
                }
            }
        }
        {
            const float rl = __builtin_amdgcn_rcpf(hihalf(o[12]));
#pragma unroll
            for (int j = 0; j < 16; ++j) o[j] *= rl;
            const u32x4 f0 = {pk(o[0], o[1]), pk(o[2], o[3]), pk(o[4], o[5]), pk(o[6], o[7])};
            u32x4 f1 = {pk(o[8], o[9]), pk(o[10], o[11]), pk(o[12], o[13]), pk(o[14], o[15])};
            if (hh) { f1[2] = 0u; f1[3] = 0u; }
            const unsigned dst = WS_OF + (unsigned)((qt * 4 + wv) * 16 + head) * 2048 + lane * 16;
            st_sc1(rws, dst, f0); st_sc1(rws, dst + 1024, f1);
        }
        WAITVM(0);
        if (lane == 0) __hip_atomic_store((GAS unsigned*)(ws + CT_F2 + ((qt * 4 + wv) * 16 + head) * FST), tag, RLX_AGENT);
        rolebar(barA, bart, lane);
        if (jx < 28) {
            const int tt = 4 * xcd + (jx & 3), nb = jx >> 2;
            const int row = lane >> 3; const unsigned voff = (unsigned)(row * 896 + nb * 128 + (((lane & 7) ^ (((row >> 1) & 1) << 2)) << 4));
#pragma unroll 1
            for (int i = 0; i < 14; ++i) { const int pc = i * 4 + wv; glds16(P.wo + pc * 8 * 896, voff, lds0 + pc * 1024); }
            if (wv == 1 && lane < 32) *(LDSA u32x4*)(sm + 448 * 128 + lane * 16) = (u32x4){0u, 0u, 0u, 0u};
            const int qg = wv & 1, kh = wv >> 1;
            WAITVM(0);
            rolebar(barA, bart, lane);
            bf16x8 bk[16], bs[16];
            {
                const int q4 = (lane & 15) >> 2;
                const int chunk = (kh * 4 + 2 * ((lane >> 4) & 1) + ((lane & 3) >> 1)) ^ (((q4 >> 1) & 1) << 2);
                LDSA const char* bp = sm + (4 * hh + q4 + kh * 8 * 28) * 128 + 8 * (lane & 1);
                LDSA const char* bkp = bp + chunk * 16; LDSA const char* bsp = bp + (chunk ^ 4) * 16;
#pragma unroll
                for (int i = 0; i < 16; ++i) { const int ro = (28 * (i >> 1) + 16 * (i & 1)) * 128; bk[i] = cat(trread(bkp + ro), trread(bkp + ro + 1024)); bs[i] = cat(trread(bsp + ro), trread(bsp + ro + 1024)); }
            }
            ldswait(tagw + 4, 1u);
            SBAR();
            pollflags(ws, CT_F2 + ((tt * 2 + qg) * 16 + kh * 8 + (lane & 7)) * FST, lane < 8, tag);
            u32x4 a[16];
#pragma unroll
            for (int i = 0; i < 16; ++i) a[i] = ld_sc1(rws, WS_OF + (unsigned)((tt * 2 + qg) * 16 + kh * 8) * 2048 + i * 1024 + lane * 16);
            SBAR();
            f32x16 c = {}, cs = {};
#pragma unroll
            for (int i = 0; i < 16; ++i) { cs = MFMA(asfrag(a[i]), bs[i], cs); c = MFMA(asfrag(a[i]), bk[i], c); }
            LDSA f32x4* xch = (LDSA f32x4*)(sm + L_XCH) + wv * 256;
#pragma unroll
            for (int j = 0; j < 4; ++j) xch[j * 64 + lane] = (f32x4){cs[4 * j], cs[4 * j + 1], cs[4 * j + 2], cs[4 * j + 3]};
            rolebar(barA, bart, lane);
            { LDSA const f32x4* rcv = (LDSA const f32x4*)(sm + L_XCH) + (wv ^ 2) * 256;
              f32x4 t[4];
#pragma unroll
              for (int j = 0; j < 4; ++j) t[j] = rcv[j * 64 + lane];
#pragma unroll
              for (int j = 0; j < 4; ++j) { c[4 * j] += t[j][0]; c[4 * j + 1] += t[j][1]; c[4 * j + 2] += t[j][2]; c[4 * j + 3] += t[j][3]; } }
            store_tile(__builtin_amdgcn_make_buffer_rsrc(P.out, (short)0, 2048 * 3584, 0x00020000), (unsigned)((tt * 2 + qg) * 32) * 3584 + (448 + nb * 64 + kh * 32) * 4, c, sm + wv * 4096, lane);
        }
    } else {
        __builtin_amdgcn_s_setprio(1);
        {
            const int rt = (xcd & 3) * 4 + (jx & 3), g = (xcd >> 2) * 8 + (jx >> 2);
            const char* x = P.xB; const char* w = P.wqkv;
            unsigned xoff[4], woff[3];
#pragma unroll
            for (int q = 0; q < 4; ++q) xoff[q] = (unsigned)((rt * 128 + 32 * wv + 8 * q + (lane >> 3)) * 896 + (((lane & 7) ^ ((4 * q + (lane >> 4)) & 7)) << 4));
#pragma unroll
            for (int i = 0; i < 3; ++i) woff[i] = (unsigned)((16 * wv + (lane >> 2)) * 2688 + 896 * i + 56 * g - (i == 2 && g == 15 ? 8 : 0) + (lane & 3) * 16);
            auto issue = [&](int kc, int slot) {
                const unsigned sb = lds0 + slot * QKV_SLOT;
                if (wv & 1) {
#pragma unroll
                    for (int i = 0; i < 3; ++i) glds16(w + kc * 64 * 2688, woff[i], sb + 16384 + i * 4096 + wv * 1024);
                }
#pragma unroll
                for (int q = 0; q < 4; ++q) glds16(x + kc * 128, xoff[q], sb + (4 * wv + q) * 1024);
                if (!(wv & 1))
                {
#pragma unroll
                    for (int i = 0; i < 3; ++i) glds16(w + kc * 64 * 2688, woff[i], sb + 16384 + i * 4096 + wv * 1024);
                }
            };
            const int k0 = (g + 2 * rt) % 7;
            auto rotk = [&](int i) { const int k = i + k0; return k >= 7 ? k - 7 : k; };
            issue(rotk(0), 0);
            issue(rotk(1), 1);
            HWBAR();
#pragma unroll 1
            for (int kc = 2; kc < 7; ++kc) {
                if (kc >= 5) ldswait4(qfree, kc - 4);
                issue(rotk(kc), kc < 5 ? kc : kc - 5);
                if (kc < 6) { WAITVM(14); __hip_atomic_store(qfull + wv, (unsigned)(kc - 1), RLX_WG); }
            }
            {
                const unsigned xo = (unsigned)((xcd & 3) * 458752 + jx * 14336 + lane * 16), bo = (unsigned)((xcd >> 2) * 200704 + jx * 6272 + lane * 16);
#pragma unroll
                for (int i = 0; i < 4; ++i) glds16(P.xA + (wv + 4 * i < 14 ? wv + 4 * i : wv) * 1024, xo, lds0 + L_DUMMY);
#pragma unroll
                for (int i = 0; i < 3; ++i) { const int pc = wv * 3 + i; glds16((pc < 6 ? P.Bre : P.Bim - 6144) + pc * 1024, bo, lds0 + L_DUMMY); }
            }
            WAITVM(21); __hip_atomic_store(qfull + wv, 5u, RLX_WG);
            WAITVM(14); __hip_atomic_store(qfull + wv, 6u, RLX_WG);
            WAITVM(7); __hip_atomic_store(qfull + wv, 7u, RLX_WG);
        }
        const bool has = jx < 28;
        if (has) {
            const int c = 4 * (xcd & 3) + (jx & 3), gp = 7 * (xcd >> 2) + (jx >> 2);
            const int sw = (r & 7) * 128 + ((hh ^ ((r >> 1) & 7)) << 4);
            ldswait(tagw, 1u);
            tag = __builtin_amdgcn_readfirstlane(__hip_atomic_load(tagw + 1, RLX_WG));
            ldswait4(qfree, 5u);
            f32x16 are, aim;
            {
                unsigned xoff[4];
#pragma unroll
                for (int q = 0; q < 4; ++q) xoff[q] = (unsigned)((c * 128 + 32 * wv + 8 * q + (lane >> 3)) * 896 + (((lane & 7) ^ ((4 * q + (lane >> 4)) & 7)) << 4));
                const unsigned boff = (unsigned)((gp * 32 + 8 * wv + (lane >> 3)) * 896 + (((lane & 7) ^ ((4 * wv + (lane >> 4)) & 7)) << 4));
                const int tr = 16 * ((r >> 2) & 1) + (r & 3) + 4 * (r >> 3);
                const unsigned xa = lds0 + L_BU + (4 * wv + (tr >> 3)) * 1024 + (tr & 7) * 128 + ((hh ^ ((tr >> 1) & 7)) << 4), ba = lds0 + L_BU + 16384 + (r >> 3) * 1024 + sw;
                asm volatile(BU_ASM : "=&{v[16:31]}"(are), "=&{v[32:47]}"(aim)
                    : [x0] "v"(xa), [x1] "v"(xa ^ 32u), [x2] "v"(xa ^ 64u), [x3] "v"(xa ^ 96u), [b0] "v"(ba), [b1] "v"(ba ^ 32u), [b2] "v"(ba ^ 64u), [b3] "v"(ba ^ 96u),
                      [xo0] "v"(xoff[0]), [xo1] "v"(xoff[1]), [xo2] "v"(xoff[2]), [xo3] "v"(xoff[3]), [bo] "v"(boff), [ct] "v"(lds0 + L_CTL + 80u), [me] "v"(lds0 + L_CTL + 80u + wv * 4),
                      [xa] "s"(P.xA), [bre] "s"(P.Bre), [bim] "s"(P.Bim), [k0] "s"((c + 2 * gp) % 7), [lx] "s"(lds0 + L_BU + wv * 4096), [lb] "s"(lds0 + L_BU + 16384 + wv * 1024)
                    : BU_CLOBBERS);
            }
            ldswait(tagw + 2, 1u);
            cf lam, l16, l32, l128, c2;
            { LDSA const f32x2* co = (LDSA const f32x2*)(sm + L_SX + 1536) + r; f32x2 t;
              t = co[0]; lam = {t[0], t[1]}; t = co[32]; l16 = {t[0], t[1]}; t = co[64]; l32 = {t[0], t[1]}; t = co[96]; l128 = {t[0], t[1]}; t = co[128]; c2 = {t[0], t[1]}; }
            cf e16 = {are[0], aim[0]};
#pragma unroll
            for (int j = 1; j < 16; ++j) e16 = cfma(lam, e16, cf{are[j], aim[j]});
            const cf elo = {lohalf(e16.re), lohalf(e16.im)};
            const cf Tw = cfma(l16, elo, cf{hihalf(e16.re), hihalf(e16.im)});
            LDSA f32x2* ex = (LDSA f32x2*)(sm + L_SX);
            if (hh == 0) ex[wv * 32 + r] = (f32x2){Tw.re, Tw.im};
            rolebar<4>(barS, bart, lane);
            if (wv == 0 && lane == 0) __hip_atomic_store(tagw + 4, 1u, RLX_WG);
            cf Cw = {0.f, 0.f};
            for (int w2 = 0; w2 < wv; ++w2) { const f32x2 t = ex[w2 * 32 + r]; Cw = cfma(l32, Cw, cf{t[0], t[1]}); }
            GAS unsigned long long* eg = (GAS unsigned long long*)(ws + CT_EG) + (size_t)gp * 16 * 64 + lane;
            if (wv == 3) {
                const cf E = cfma(l32, Cw, Tw);
                __hip_atomic_store(eg + c * 64, ((unsigned long long)tag << 32) | asu(hh ? E.im : E.re), RLX_AGENT);
            }
            const int cy_ = 2 * xcd + (jx & 1), nb = jx >> 1;
            if (wv != 0) {
#pragma unroll 1
                for (int q = wv - 1; q < 56; q += 3) {
                    const int ch = q >> 2, pc = q & 3, row32 = pc * 8 + (lane >> 3);
                    const unsigned coff = (unsigned)((nb * 32 + row32) * 896 + (((lane & 7) ^ ((row32 >> 1) & 7)) << 4));
                    glds16((ch < 7 ? P.Cre : P.Cim - 896) + ch * 128, coff, lds0 + L_S + 20480 + ch * 4096 + pc * 1024);
                    __builtin_amdgcn_s_sleep(2);
                }
            }
            if (wv == 0) {
                unsigned long long v[15];
#pragma unroll 1
                for (unsigned spins = 0; spins < SPIN_CAP; ++spins) {
                    bool ok = true;
#pragma unroll
                    for (int c2_ = 0; c2_ < 15; ++c2_) { v[c2_] = c2_ < c ? __hip_atomic_load(eg + c2_ * 64, RLX_AGENT) : (unsigned long long)tag << 32; }
#pragma unroll
                    for (int c2_ = 0; c2_ < 15; ++c2_) ok = ok && (unsigned)(v[c2_] >> 32) == tag;
                    if (!anylane(!ok)) break;
                    __builtin_amdgcn_s_sleep(4);
                }
                cf Cin = {0.f, 0.f};
#pragma unroll
                for (int c2_ = 0; c2_ < 15; ++c2_) if (c2_ < c) { const float e = asf((unsigned)v[c2_]); Cin = cfma(l128, Cin, cf{lohalf(e), hihalf(e)}); }
                if (hh == 0) ex[128 + r] = (f32x2){Cin.re, Cin.im};
            }
            rolebar<4>(barS, bart, lane);
            { cf pw32 = {1.f, 0.f}; for (int w2 = 0; w2 < wv; ++w2) pw32 = cmul(pw32, l32); const f32x2 t = ex[128 + r]; Cw = cfma(pw32, cf{t[0], t[1]}, Cw); }
            LDSA char* img = sm + L_S + wv * 5120;
            {
                cf h = hh ? cfma(l16, Cw, elo) : Cw;
                unsigned pr[8], pi[8];
#pragma unroll
                for (int j = 0; j < 16; j += 2) {
                    h = cfma(lam, h, cf{are[j], aim[j]}); const cf z0 = cmul(c2, h);
                    h = cfma(lam, h, cf{are[j + 1], aim[j + 1]}); const cf z1 = cmul(c2, h);
                    pr[j >> 1] = pk(z0.re, z1.re); pi[j >> 1] = pk(-z0.im, -z1.im);
                }
                *(LDSA u32x4*)(img + r * 80 + 32 * hh) = (u32x4){pr[0], pr[1], pr[2], pr[3]}; *(LDSA u32x4*)(img + r * 80 + 32 * hh + 16) = (u32x4){pr[4], pr[5], pr[6], pr[7]};
                *(LDSA u32x4*)(img + 2560 + r * 80 + 32 * hh) = (u32x4){pi[0], pi[1], pi[2], pi[3]}; *(LDSA u32x4*)(img + 2560 + r * 80 + 32 * hh + 16) = (u32x4){pi[4], pi[5], pi[6], pi[7]};
            }
            WAITLGKM();
            const int ta = (8 * hh + ((lane & 15) >> 2)) * 80 + (16 * ((lane >> 4) & 1) + 4 * (lane & 3)) * 2;
#pragma unroll
            for (int part = 0; part < 2; ++part)
#pragma unroll
                for (int s = 0; s < 2; ++s) {
                    const bf16x8 f = cat(trread(img + part * 2560 + ta + s * 16 * 80), trread(img + part * 2560 + ta + s * 16 * 80 + 4 * 80));
                    st_sc1(rws, WS_HF + (unsigned)((c * 4 + wv) * 56 + part * 28 + 2 * gp + s) * 1024 + lane * 16, __builtin_bit_cast(u32x4, f));
                }
            WAITVM(0);
            rolebar<4>(barS, bart, lane);
            if (wv == 0 && lane == 0) __hip_atomic_store((GAS unsigned*)(ws + CT_F3 + (c * 16 + gp) * FST), tag, RLX_AGENT);
            f32x16 acc;
            {
                const char* xp = P.xA + (size_t)((cy_ * 4 + wv) * 32 + r) * 896 + nb * 64 + hh * 16;
                const u32x4 x0 = *(const u32x4*)xp, x1 = *(const u32x4*)(xp + 32);
                const unsigned dk = (unsigned)P.Dsk[nb * 32 + r] << (16 * (r & 1));
                const int sl = (r & 7) >> 1, kb = r >> 3;
                u32x4 d0, d1;
#pragma unroll
                for (int i = 0; i < 4; ++i) { d0[i] = (kb == hh && sl == i) ? dk : 0u; d1[i] = (kb == 2 + hh && sl == i) ? dk : 0u; }
                const f32x16 zero = {};
                acc = MFMA(asfrag(x0), asfrag(d0), zero); acc = MFMA(asfrag(x1), asfrag(d1), acc);
            }
            {
#pragma unroll
                for (int i = 0; i < 4; ++i) { const int o_ = i * 1024 + lane * 16; glds16(P.wo, (unsigned)(jx * 14336 + wv * 3584 + (o_ < 3584 ? o_ : 0)), lds0 + L_DUMMY); } }
            WAITVM(0);
            rolebar<4>(barS, bart, lane);
            {
                const unsigned ba2 = lds0 + L_S + 20480 + (r >> 3) * 1024 + sw;
                const char* hb = ws + WS_HF + (size_t)((cy_ * 4 + wv) * 56) * 1024;
                asm volatile(Y_ASM : "+{v[240:255]}"(acc)
                    : [lo] "v"(lane * 16), [c0] "v"(ba2), [hb] "s"(hb), [ae] "v"(lds0 + L_CTL + 180u),
                      [fa] "v"(CT_F3 + (cy_ * 16 + (lane & 15)) * FST), [fb] "s"(ws), [tg] "s"(tag) : Y_CLOBBERS);
            }
            {
                const int l2_ = __builtin_amdgcn_mbcnt_hi(-1, __builtin_amdgcn_mbcnt_lo(-1, 0));
                store_tile(__builtin_amdgcn_make_buffer_rsrc(P.out, (short)0, 2048 * 3584, 0x00020000), (unsigned)((cy_ * 4 + wv) * 32) * 3584 + nb * 128, acc, sm + L_S + wv * 5120, l2_);
            }
        }
        if (b == 0 && wv == 0) {
            ldswait(tagw, 1u);
            tag = __builtin_amdgcn_readfirstlane(__hip_atomic_load(tagw + 1, RLX_WG));
            const int lane = __builtin_amdgcn_mbcnt_hi(-1, __builtin_amdgcn_mbcnt_lo(-1, 0));
            GAS unsigned* sp = (GAS unsigned*)(ws + CT_START) + lane;
#pragma unroll 1
            for (unsigned spins = 0; spins < SPIN_CAP; ++spins) {
                bool ok = true;
#pragma unroll
                for (int i = 0; i < 4; ++i) ok = ok && __hip_atomic_load(sp + i * 64, RLX_AGENT) == tag;
                if (!anylane(!ok)) break;
                __builtin_amdgcn_s_sleep(2);
            }
            if (lane == 0) __hip_atomic_store((GAS unsigned*)(ws + CT_GEN), tag, RLX_AGENT);
        }
    }
}

#if defined(__HIP_DEVICE_COMPILE__)
#pragma clang attribute pop
#endif

extern "C" void kernel_launch(void* const* d_in, const int* in_sizes, int n_in, void* d_out, int out_size, void* d_ws, size_t ws_size, hipStream_t stream) {
    Params p;
    p.xA = (const char*)d_in[0]; p.Lre = (const unsigned short*)d_in[1]; p.Lim = (const unsigned short*)d_in[2];
    p.Bre = (const char*)d_in[3]; p.Bim = (const char*)d_in[4]; p.Cre = (const char*)d_in[5]; p.Cim = (const char*)d_in[6];
    p.ldt = (const unsigned short*)d_in[7]; p.Dsk = (const unsigned short*)d_in[8];
    p.xB = (const char*)d_in[9]; p.wqkv = (const char*)d_in[10]; p.wo = (const char*)d_in[11];
    p.out = (float*)d_out; p.ws = (char*)d_ws;
    (void)hipFuncSetAttribute((const void*)fused, hipFuncAttributeMaxDynamicSharedMemorySize, LDS_BYTES);
    fused<<<256, 512, LDS_BYTES, stream>>>(p);
}
```
